```python
import torch

def _softmax(x, axis=-1):
    return torch.softmax(x, dim=axis)
def _gelu(x):
    return torch.nn.functional.gelu(x, approximate="tanh")
def _silu(x):
    return torch.nn.functional.silu(x)
def _sigmoid(x):
    return torch.sigmoid(x)
def _layernorm(x, axis=-1, eps=1e-5):
    m = x.mean(dim=axis, keepdim=True)
    v = x.var(dim=axis, keepdim=True, unbiased=False)
    return (x - m) / torch.sqrt(v + eps)
def _scatter_add(tgt, idx, src, axis=0):
    return tgt.clone().index_add_(axis, idx, src)
def _segment_sum(src, seg, n):
    out = torch.zeros((n,) + src.shape[1:], dtype=src.dtype)
    return out.index_add_(0, seg, src)
def _pad(x, pw):
    flat = tuple(v for pair in reversed(pw) for v in pair)
    return torch.nn.functional.pad(x, flat)

kernel_name = 'parallel_sum__sliding_block__dense_activation_relu__prompt'

def setup_inputs():

    _w = (lambda a: (lambda t: t.to(torch.bfloat16) if t.is_floating_point() else t)(torch.as_tensor(a)))
    def _A_setup():
        B, S, D = 1, 2048, 512
        return {
            "x": _w((lambda *s: torch.randn(*s, dtype=torch.bfloat16) * 0.05)(B, S, D)),
            "wqkv": _w((lambda *s: torch.randn(*s, dtype=torch.bfloat16) * 0.05)(D, 3 * D)),
            "wo": _w((lambda *s: torch.randn(*s, dtype=torch.bfloat16) * 0.1)(D, D)),
        }
    _A_in = _A_setup()

    def _B_setup():
        B, S, D, Dff = 1, 2048, 512, 1792
        return {
            "x": _w((lambda *s: torch.randn(*s, dtype=torch.bfloat16) * 0.05)(B, S, D)),
            "w1": _w((lambda *s: torch.randn(*s, dtype=torch.bfloat16) * 0.05)(D, Dff)),
            "w2": _w((lambda *s: torch.randn(*s, dtype=torch.bfloat16) * 0.05)(Dff, D)),
        }
    _B_in = _B_setup()

    return {
        "A_x": _A_in["x"],
        "A_wqkv": _A_in["wqkv"],
        "A_wo": _A_in["wo"],
        "B_x": _B_in["x"],
        "B_w1": _B_in["w1"],
        "B_w2": _B_in["w2"],
    }

def reference(A_x, A_wqkv, A_wo, B_x, B_w1, B_w2):
    def _A_ref(x, wqkv, wo):
        B, S, D = x.shape
        H = 32; Dh = D // H; W = 256
        qkv = (lambda x, *d: x.permute(*d))((x @ wqkv).reshape(B, S, 3, H, Dh), 2, 0, 3, 1, 4)
        q, k, v = qkv[0], qkv[1], qkv[2]
        scores = torch.einsum("bhqd,bhkd->bhqk", q, k) * (Dh ** -0.5)
        idx = torch.arange(S)
        mask = (idx[:, None] - idx[None, :] >= 0) & (idx[:, None] - idx[None, :] < W)
        scores = torch.where(mask[None, None], scores, -1e9)
        p = _softmax(scores, axis=-1)
        o = torch.einsum("bhqk,bhkd->bhqd", p, v)
        return (lambda x, *d: x.permute(*d))(o, 0, 2, 1, 3).reshape(B, S, D) @ wo

    def _B_ref(x, w1, w2):
        h = torch.einsum("bsd,df->bsf", x, w1)
        zero = torch.zeros_like(h)
        h = torch.maximum(h, zero)
        return torch.einsum("bsf,fd->bsd", h, w2)

    x = A_x
    xn = _layernorm(x, axis=-1)
    return x + _A_ref(xn, A_wqkv, A_wo) + _B_ref(xn, B_w1, B_w2)
```

```cpp
#include <hip/hip_runtime.h>
#include <hip/hip_bf16.h>
typedef __hip_bfloat16 bf16;
constexpr int S=2048, D=512, DFF=1792, H=32, DH=16, W=256;
__device__ inline float b2f(bf16 v){ return __bfloat162float(v);}
__global__ void ln_k(const bf16* x, bf16* xn){
  int r=blockIdx.x; int t=threadIdx.x;
  float v[8]; float s=0;
  for(int j=0;j<8;j++){ v[j]=b2f(x[r*D+t*8+j]); s+=v[j]; }
  for(int o=32;o>=1;o>>=1) s+=__shfl_xor(s,o);
  float m=s/D; float q=0;
  for(int j=0;j<8;j++){ float d=v[j]-m; q+=d*d; }
  for(int o=32;o>=1;o>>=1) q+=__shfl_xor(q,o);
  float rs=rsqrtf(q/D+1e-5f);
  for(int j=0;j<8;j++) xn[r*D+t*8+j]=__float2bfloat16((v[j]-m)*rs);
}
__global__ void gemm_k(const bf16* A,int lda,const bf16* B,int ldb,bf16* C,int ldc,int K,int relu){
  int n=blockIdx.x*blockDim.x+threadIdx.x; int m=blockIdx.y;
  float acc=0;
  for(int k=0;k<K;k++) acc+=b2f(A[m*lda+k])*b2f(B[k*ldb+n]);
  if(relu) acc=fmaxf(acc,0.f);
  C[m*ldc+n]=__float2bfloat16(acc);
}
__global__ void attn_k(const bf16* qkv,bf16* o){
  int idx=blockIdx.x*blockDim.x+threadIdx.x; int h=idx%H; int q=idx/H;
  float qv[DH]; for(int d=0;d<DH;d++) qv[d]=b2f(qkv[q*3*D+h*DH+d]);
  int k0=max(0,q-W+1); float mx=-1e30f;
  for(int k=k0;k<=q;k++){ float s=0; for(int d=0;d<DH;d++) s+=qv[d]*b2f(qkv[k*3*D+D+h*DH+d]); mx=fmaxf(mx,s*0.25f);}
  float l=0; float acc[DH]; for(int d=0;d<DH;d++) acc[d]=0;
  for(int k=k0;k<=q;k++){ float s=0; for(int d=0;d<DH;d++) s+=qv[d]*b2f(qkv[k*3*D+D+h*DH+d]); float p=__expf(s*0.25f-mx); l+=p; for(int d=0;d<DH;d++) acc[d]+=p*b2f(qkv[k*3*D+2*D+h*DH+d]); }
  for(int d=0;d<DH;d++) o[q*D+h*DH+d]=__float2bfloat16(acc[d]/l);
}
__global__ void out_k(const bf16* x,const bf16* o,const bf16* wo,const bf16* hh,const bf16* w2,bf16* out){
  int n=blockIdx.x*blockDim.x+threadIdx.x; int m=blockIdx.y;
  float a=0,b=0;
  for(int k=0;k<D;k++) a+=b2f(o[m*D+k])*b2f(wo[k*D+n]);
  for(int k=0;k<DFF;k++) b+=b2f(hh[m*DFF+k])*b2f(w2[k*D+n]);
  out[m*D+n]=__float2bfloat16(b2f(x[m*D+n])+a+b);
}
extern "C" void kernel_launch(void* const* d_in, const int* in_sizes, int n_in, void* d_out, int out_size, void* d_ws, size_t ws_size, hipStream_t stream){
  const bf16* x=(const bf16*)d_in[0]; const bf16* wqkv=(const bf16*)d_in[1]; const bf16* wo=(const bf16*)d_in[2];
  const bf16* w1=(const bf16*)d_in[4]; const bf16* w2=(const bf16*)d_in[5];
  bf16* xn=(bf16*)d_ws; bf16* qkv=xn+S*D; bf16* hh=qkv+S*3*D; bf16* o=hh+S*DFF;
  ln_k<<<S,64,0,stream>>>(x,xn);
  gemm_k<<<dim3(3*D/256,S),256,0,stream>>>(xn,D,wqkv,3*D,qkv,3*D,D,0);
  gemm_k<<<dim3(DFF/256,S),256,0,stream>>>(xn,D,w1,DFF,hh,DFF,D,1);
  attn_k<<<S*H/256,256,0,stream>>>(qkv,o);
  out_k<<<dim3(D/256,S),256,0,stream>>>(x,o,wo,hh,w2,(bf16*)d_out);
}
```

```cpp
#include <hip/hip_runtime.h>

__global__ __launch_bounds__(512, 2) void fused_block_kernel(const void* x, const void* wqkv, const void* wo, const void* w1, const void* w2,
                                                             void* out, void* ws) {
  __shared__ __attribute__((aligned(1024))) char lds[163840];
  unsigned tid = threadIdx.x;
  asm volatile(
    "; ---- prologue\n"
    "s_load_dwordx2 s[4:5], %1, 0\n"
    "s_load_dwordx2 s[8:9], %1, 8\n"
    "s_load_dwordx2 s[28:29], %1, 48\n"
    "s_load_dwordx2 s[20:21], %1, 16\n"
    "s_load_dwordx2 s[12:13], %1, 24\n"
    "s_load_dwordx2 s[16:17], %1, 32\n"
    "s_load_dwordx2 s[24:25], %1, 40\n"
    "s_mov_b32 s32, %2\n"
    "v_readfirstlane_b32 s33, v0\n"
    "v_and_b32 v0, 63, v0\n"
    "s_and_b32 s66, s32, 1\n"
    "s_lshl_b32 s66, s66, 3\n"
    "s_lshr_b32 s33, s33, 6\n"
    "s_lshr_b32 s67, s32, 5\n"
    "s_add_u32 s34, s66, s67\n"
    "s_and_b32 s36, s33, 3\n"
    "s_lshr_b32 s37, s33, 2\n"
    "s_lshr_b32 s66, s32, 3\n"
    "s_xor_b32 s66, s66, s67\n"
    "s_bitcmp1_b32 s66, 0\n"
    "s_mov_b32 s0, 0x75634120\n"
    "s_cselect_b32 s0, 0x67452301, s0\n"
    "s_lshl_b32 s66, s32, 2\n"
    "s_add_u32 s66, s66, s32\n"
    "s_and_b32 s66, s66, 6\n"
    "s_mul_i32 s66, s66, 0x11111111\n"
    "s_add_u32 s0, s0, s66\n"
    "s_and_b32 s0, s0, 0x77777777\n"
    "v_lshrrev_b32 v23, 4, v0\n"
    "v_xor_b32 v23, v23, v0\n"
    "v_and_b32 v23, 7, v23\n"
    "v_lshlrev_b32 v23, 4, v23\n"
    "v_lshrrev_b32 v28, 3, v0\n"
    "v_lshl_or_b32 v2, v28, 10, v23\n"
    "v_xor_b32 v28, 0x2040, v2\n"
    "s_lshl_b32 s66, s36, 1\n"
    "s_add_u32 s66, s66, s37\n"
    "s_lshl_b32 s41, s66, 11\n"
    "s_add_u32 s41, s41, 0xc000\n"
    "s_lshl_b32 s67, s34, 3\n"
    "s_add_u32 s66, s66, s67\n"
    "s_lshl_b32 s40, s66, 14\n"
    "s_mov_b32 s6, 0x200000\n"
    "s_mov_b32 s7, 0x20000\n"
    "s_mov_b32 s10, 0x180000\n"
    "s_mov_b32 s11, 0x20000\n"
    "s_bfe_u32 s2, s0, 0x40000\n"
    "s_lshl_b32 s2, s2, 7\n"
    "s_add_u32 s2, s2, s40\n"
    "s_mov_b32 m0, s41\n"
    "s_waitcnt lgkmcnt(0)\n"
    "buffer_load_dwordx4 v2, s[4:7], s2 offen lds\n"
    "s_add_u32 m0, s41, 0x400\n"
    "s_nop 0\n"
    "buffer_load_dwordx4 v28, s[4:7], s2 offen lds\n"
    "s_load_dword s38, s[28:29], 0\n"
    "s_lshr_b32 s68, s32, 3\n"
    "s_lshl_b32 s68, s68, 3\n"
    "s_add_u32 s44, s68, s33\n"
    "s_bfe_u32 s66, s32, 0x20001\n"
    "s_lshl_b32 s66, s66, 2\n"
    "s_bfe_u32 s67, s32, 0x20003\n"
    "s_add_u32 s35, s66, s67\n"
    "v_bfe_u32 v55, v0, 4, 1\n"
    "s_and_b32 s66, s33, 1\n"
    "s_lshl_b32 s66, s66, 1\n"
    "v_or_b32 v55, s66, v55\n"
    "v_lshlrev_b32 v55, 1, v55\n"
    "v_xor_b32 v55, v55, v0\n"
    "v_and_b32 v55, 7, v55\n"
    "v_lshrrev_b32 v23, 3, v0\n"
    "v_mul_u32_u24 v7, 0xc00, v23\n"
    "v_lshl_add_u32 v7, v55, 4, v7\n"
    "s_lshl_b32 s45, s33, 10\n"
    "s_and_b32 s39, s35, 1\n"
    "s_mul_i32 s66, s33, 24\n"
    "s_add_u32 s66, s66, s39\n"
    "s_lshl_b32 s66, s66, 10\n"
    "s_lshr_b32 s67, s35, 1\n"
    "s_lshl_b32 s67, s67, 7\n"
    "s_add_u32 s42, s66, s67\n"
    "s_add_u32 s42, s42, 0x400\n"
    "s_bfe_u32 s71, s0, 0x40000\n"
    "s_mul_i32 s71, s71, 0x30000\n"
    "s_mov_b32 m0, s45\n"
    "s_add_u32 s71, s71, s42\n"
    "buffer_load_dwordx4 v7, s[8:11], s71 offen lds\n"
    "v_lshrrev_b32 v31, 2, v0\n"
    "v_mul_u32_u24 v31, 0xc00, v31\n"
    "v_bfe_u32 v23, v0, 1, 1\n"
    "v_lshl_add_u32 v31, v23, 10, v31\n"
    "v_and_b32 v23, 1, v0\n"
    "v_lshl_add_u32 v31, v23, 7, v31\n"
    "s_mul_i32 s66, s44, 0x1800\n"
    "s_lshr_b32 s67, s35, 2\n"
    "s_lshl_b32 s67, s67, 8\n"
    "s_add_u32 s66, s66, s67\n"
    "s_add_u32 s66, s66, 0x400\n"
    "s_mov_b64 exec, 0xff\n"
    "buffer_load_dword v124, v31, s[8:11], s66 offen\n"
    "s_mov_b64 exec, -1\n"
    "s_mov_b32 s57, s44\n"
    "s_barrier\n"
    "s_bfe_u32 s2, s0, 0x40004\n"
    "s_lshl_b32 s2, s2, 7\n"
    "s_add_u32 s2, s2, s40\n"
    "s_add_u32 m0, s41, 0x4000\n"
    "s_nop 0\n"
    "buffer_load_dwordx4 v2, s[4:7], s2 offen lds\n"
    "s_bfe_u32 s71, s0, 0x40004\n"
    "s_mul_i32 s71, s71, 0x30000\n"
    "s_add_u32 m0, s45, 0x2000\n"
    "s_add_u32 s71, s71, s42\n"
    "buffer_load_dwordx4 v7, s[8:11], s71 offen lds\n"
    "v_lshlrev_b32 v1, 4, v0\n"
    "v_lshrrev_b32 v48, 4, v0\n"
    "v_bfe_u32 v49, v0, 2, 2\n"
    "v_and_b32 v50, 3, v0\n"
    "v_bfe_u32 v23, v0, 1, 3\n"
    "v_xor_b32 v23, v23, v48\n"
    "v_lshlrev_b32 v23, 4, v23\n"
    "v_and_b32 v51, 15, v0\n"
    "v_lshl_or_b32 v23, v51, 7, v23\n"
    "s_lshl_b32 s66, s36, 12\n"
    "s_add_u32 s66, s66, 0xc000\n"
    "v_add_u32 v6, s66, v23\n"
    "v_xor_b32 v29, 64, v6\n"
    "v_lshl_add_u32 v54, v48, 3, v49\n"
    "v_lshlrev_b32 v54, 7, v54\n"
    "v_lshl_add_u32 v54, v50, 3, v54\n"
    "v_and_b32 v51, 1, v48\n"
    "v_lshlrev_b32 v53, 1, v51\n"
    "v_lshrrev_b32 v52, 1, v49\n"
    "v_or_b32 v53, v53, v52\n"
    "v_xor_b32 v52, s37, v53\n"
    "v_lshl_add_u32 v14, v52, 5, v54\n"
    "v_xor_b32 v15, 64, v14\n"
    "s_and_b32 s66, s36, 1\n"
    "s_lshl_b32 s66, s66, 6\n"
    "v_xor_b32 v8, s66, v14\n"
    "v_xor_b32 v35, s66, v15\n"
    "v_mov_b32 v24, 0x3f803f80\n"
    "v_mov_b32 v25, 0x3f803f80\n"
    "v_mov_b32 v26, 0x3f803f80\n"
    "v_mov_b32 v27, 0x3f803f80\n"
    "; ---- pass 1\n"
    "s_mov_b32 s30, 0x1100000\n"
    "s_mov_b32 s31, 0x20000\n"
    "s_mov_b32 s14, 0x1c0000\n"
    "s_mov_b32 s15, 0x20000\n"
    "s_mov_b32 s18, 0x1c0000\n"
    "s_mov_b32 s19, 0x20000\n"
    "s_mov_b32 s22, 0x80000\n"
    "s_mov_b32 s23, 0x20000\n"
    "s_mov_b32 s26, 0x200000\n"
    "s_mov_b32 s27, 0x20000\n"
    "s_lshl_b32 s66, s32, 3\n"
    "s_add_u32 s66, s66, s33\n"
    "s_lshl_b32 s66, s66, 10\n"
    "v_lshl_add_u32 v30, v0, 7, s66\n"
    "s_lshl_b32 s47, s34, 2\n"
    "s_add_u32 s47, s47, s36\n"
    "s_lshl_b32 s48, s35, 1\n"
    "s_add_u32 s48, s48, s37\n"
    "s_lshl_b32 s55, s47, 9\n"
    "s_add_u32 s55, s55, 0x28000\n"
    "s_lshl_b32 s1, s37, 2\n"
    "s_lshr_b32 s1, s0, s1\n"
    "s_mul_i32 s43, s36, 0xc000\n"
    "s_lshl_b32 s66, s35, 6\n"
    "s_add_u32 s43, s43, s66\n"
    "v_lshrrev_b32 v39, 5, v0\n"
    "v_lshlrev_b32 v39, 1, v39\n"
    "v_xor_b32 v39, v39, v0\n"
    "v_and_b32 v39, 3, v39\n"
    "v_lshrrev_b32 v3, 2, v0\n"
    "v_mul_u32_u24 v3, 0xc00, v3\n"
    "v_lshl_add_u32 v3, v39, 4, v3\n"
    "v_lshrrev_b32 v38, 3, v0\n"
    "v_mul_u32_u24 v4, 0xe00, v38\n"
    "v_lshl_add_u32 v4, v55, 4, v4\n"
    "s_and_b32 s66, s35, 3\n"
    "s_lshr_b32 s67, s35, 2\n"
    "s_mul_i32 s67, s67, 0x380\n"
    "s_mul_i32 s68, s33, 0x7000\n"
    "s_add_u32 s67, s67, s68\n"
    "s_lshl_b32 s68, s66, 8\n"
    "s_add_u32 s46, s67, s68\n"
    "s_lshl_b32 s68, s66, 3\n"
    "s_lshr_b32 s69, 0x140e0604, s68\n"
    "s_and_b32 s69, s69, 0xff\n"
    "s_lshl_b32 s68, s69, 5\n"
    "s_add_u32 s3, s67, s68\n"
    "s_lshr_b32 s67, 6, s66\n"
    "s_and_b32 s67, s67, 1\n"
    "v_lshrrev_b32 v39, 2, v55\n"
    "s_lshl_b32 s68, s67, 7\n"
    "v_mad_u32_u24 v10, v39, s68, v4\n"
    "s_mul_i32 s64, s47, 0x90\n"
    "s_lshr_b32 s68, s35, 2\n"
    "s_mul_i32 s68, s68, 28\n"
    "s_add_u32 s68, s68, s64\n"
    "s_lshl_b32 s50, s66, 3\n"
    "s_add_u32 s50, s50, s37\n"
    "s_add_u32 s50, s50, s68\n"
    "s_lshl_b32 s50, s50, 10\n"
    "s_add_u32 s50, s50, 0x600000\n"
    "s_add_u32 s69, s69, s68\n"
    "s_lshl_b32 s67, s67, 2\n"
    "s_add_u32 s67, s67, 2\n"
    "s_add_u32 s66, s37, s39\n"
    "s_lshr_b32 s68, s66, 1\n"
    "s_mul_i32 s68, s68, s67\n"
    "s_and_b32 s51, s66, 1\n"
    "s_add_u32 s51, s51, s68\n"
    "s_add_u32 s51, s51, s69\n"
    "s_lshl_b32 s51, s51, 10\n"
    "s_add_u32 s51, s51, 0x600000\n"
    "s_add_u32 s57, s69, s67\n"
    "s_add_u32 s57, s57, s39\n"
    "s_lshl_b32 s57, s57, 10\n"
    "s_add_u32 s57, s57, 0x600000\n"
    "s_add_u32 s64, s64, s48\n"
    "s_add_u32 s64, s64, 0x70\n"
    "s_lshl_b32 s64, s64, 10\n"
    "s_add_u32 s64, s64, 0x600000\n"
    "s_mov_b32 s74, 0\n"
    "s_mov_b32 s75, 0xce800000\n"
    "v_add_u32 v39, 0x27000, v1\n"
    "ds_write_b128 v39, v[24:27]\n"
    "v_lshlrev_b32 v39, 4, v48\n"
    "s_lshl_b32 s66, s33, 6\n"
    "s_add_u32 s66, s66, 0x27800\n"
    "v_add_u32 v11, s66, v39\n"
    "s_lshl_b32 s66, s37, 6\n"
    "s_add_u32 s66, s66, 0x27800\n"
    "v_add_u32 v12, s66, v39\n"
    "s_lshl_b32 s66, s39, 6\n"
    "v_add_u32 v13, s66, v12\n"
    "v_and_b32 v51, 1, v48\n"
    "v_lshlrev_b32 v39, 2, v51\n"
    "v_xor_b32 v39, v39, v0\n"
    "v_and_b32 v39, 7, v39\n"
    "v_lshlrev_b32 v39, 4, v39\n"
    "v_lshl_or_b32 v5, v38, 10, v39\n"
    "v_lshl_add_u32 v52, v51, 3, v49\n"
    "v_lshlrev_b32 v52, 6, v52\n"
    "v_lshl_add_u32 v52, v50, 3, v52\n"
    "v_lshrrev_b32 v39, 1, v48\n"
    "v_lshl_add_u32 v39, v39, 10, v52\n"
    "v_xor_b32 v52, s37, v51\n"
    "v_lshl_add_u32 v9, v52, 5, v39\n"
    "s_and_b32 s66, s36, 1\n"
    "v_xor_b32 v52, s66, v51\n"
    "v_lshl_add_u32 v39, v52, 5, v39\n"
    "s_add_u32 s66, s37, s39\n"
    "v_xor_b32 v52, s66, v53\n"
    "v_lshl_add_u32 v16, v52, 5, v54\n"
    "s_add_u32 s66, s66, 2\n"
    "s_and_b32 s66, s66, 3\n"
    "v_xor_b32 v52, s66, v53\n"
    "v_lshl_add_u32 v17, v52, 5, v54\n"
    "v_xor_b32 v52, s36, v53\n"
    "v_lshl_add_u32 v18, v52, 5, v54\n"
    "s_cmp_eq_u32 s37, 0\n"
    "s_cselect_b64 vcc, -1, 0\n"
    "v_cndmask_b32 v18, v18, v39, vcc\n"
    "s_lshl_b32 s66, s36, 12\n"
    "s_lshl_b32 s67, s37, 11\n"
    "s_add_u32 s66, s66, s67\n"
    "s_add_u32 s66, s66, 0x1c000\n"
    "v_add_u32 v19, s66, v1\n"
    "v_and_b32 v39, 31, v0\n"
    "v_lshlrev_b32 v39, 5, v39\n"
    "v_lshrrev_b32 v51, 5, v0\n"
    "v_lshl_or_b32 v39, v51, 4, v39\n"
    "s_add_u32 s66, s66, 0x400\n"
    "v_add_u32 v20, s66, v39\n"
    "s_cmp_eq_u32 s36, 3\n"
    "s_cselect_b32 s66, 0x2000, 0\n"
    "v_add_u32 v21, s66, v19\n"
    "s_lshl_b32 s49, s47, 2\n"
    "s_lshr_b32 s66, s48, 3\n"
    "s_add_u32 s49, s49, s66\n"
    "s_lshl_b32 s49, s49, 7\n"
    "s_and_b32 s66, s48, 7\n"
    "s_lshl_b32 s66, s66, 2\n"
    "s_add_u32 s49, s49, s66\n"
    "s_lshl_b32 s52, s47, 5\n"
    "s_add_u32 s52, s52, s48\n"
    "s_lshl_b32 s52, s52, 11\n"
    "s_add_u32 s52, s52, 0x100000\n"
    "s_add_u32 m0, s41, 0x4400\n"
    "s_nop 0\n"
    "buffer_load_dwordx4 v28, s[4:7], s2 offen lds\n"
    "s_lshl_b32 s66, s34, 4\n"
    "s_add_u32 s66, s66, s35\n"
    "s_lshl_b32 s66, s66, 3\n"
    "s_add_u32 s56, s66, s33\n"
    "s_lshl_b32 s66, s56, 10\n"
    "buffer_load_dwordx4 v[40:43], v1, s[4:7], s66 offen\n"
    "s_bfe_u32 s2, s0, 0x40008\n"
    "s_lshl_b32 s2, s2, 7\n"
    "s_add_u32 s2, s2, s40\n"
    "s_add_u32 m0, s41, 0x8000\n"
    "s_nop 0\n"
    "buffer_load_dwordx4 v2, s[4:7], s2 offen lds\n"
    "s_bfe_u32 s71, s0, 0x40008\n"
    "s_mul_i32 s71, s71, 0x30000\n"
    "s_add_u32 m0, s45, 0x4000\n"
    "s_add_u32 s71, s71, s42\n"
    "buffer_load_dwordx4 v7, s[8:11], s71 offen lds\n"
    "s_waitcnt vmcnt(7) lgkmcnt(0)\n"
    "s_barrier\n"
    "ds_read_b64_tr_b16 v[76:77], v8\n"
    "ds_read_b64_tr_b16 v[78:79], v8 offset:512\n"
    "ds_read_b128 v[128:131], v6\n"
    "ds_read_b128 v[132:135], v6 offset:2048\n"
    "ds_read_b64_tr_b16 v[80:81], v35\n"
    "ds_read_b64_tr_b16 v[82:83], v35 offset:512\n"
    "s_waitcnt lgkmcnt(0)\n"
    "v_mfma_f32_16x16x32_bf16 v[108:111], v[76:79], v[128:131], 0\n"
    "ds_read_b64_tr_b16 v[92:93], v8 offset:4096\n"
    "ds_read_b64_tr_b16 v[94:95], v8 offset:4608\n"
    "s_waitcnt vmcnt(2)\n"
    "v_lshlrev_b32 v56, 16, v40\n"
    "v_mfma_f32_16x16x32_bf16 v[112:115], v[76:79], v[132:135], 0\n"
    "ds_read_b128 v[136:139], v29\n"
    "ds_read_b128 v[140:143], v29 offset:2048\n"
    "s_add_u32 m0, s41, 0x8400\n"
    "s_nop 0\n"
    "buffer_load_dwordx4 v28, s[4:7], s2 offen lds\n"
    "v_and_b32 v57, 0xffff0000, v40\n"
    "v_lshlrev_b32 v58, 16, v41\n"
    "v_mfma_f32_16x16x32_bf16 v[116:119], v[80:83], v[128:131], 0\n"
    "ds_read_b64_tr_b16 v[96:97], v35 offset:4096\n"
    "ds_read_b64_tr_b16 v[98:99], v35 offset:4608\n"
    "s_bfe_u32 s2, s0, 0x4000c\n"
    "s_lshl_b32 s2, s2, 7\n"
    "s_add_u32 s2, s2, s40\n"
    "s_add_u32 m0, s41, 0xc000\n"
    "s_nop 0\n"
    "buffer_load_dwordx4 v2, s[4:7], s2 offen lds\n"
    "v_and_b32 v59, 0xffff0000, v41\n"
    "v_lshlrev_b32 v60, 16, v42\n"
    "v_mfma_f32_16x16x32_bf16 v[120:123], v[80:83], v[132:135], 0\n"
    "v_and_b32 v61, 0xffff0000, v42\n"
    "v_lshlrev_b32 v62, 16, v43\n"
    "v_mfma_f32_16x16x32_bf16 v[72:75], v[76:79], v[24:27], 0\n"
    "s_bfe_u32 s71, s0, 0x4000c\n"
    "s_mul_i32 s71, s71, 0x30000\n"
    "s_add_u32 m0, s45, 0x6000\n"
    "s_add_u32 s71, s71, s42\n"
    "buffer_load_dwordx4 v7, s[8:11], s71 offen lds\n"
    "v_and_b32 v63, 0xffff0000, v43\n"
    "v_add_f32 v64, v56, v57\n"
    "s_waitcnt lgkmcnt(0)\n"
    "s_barrier\n"
    "v_mfma_f32_16x16x32_bf16 v[108:111], v[92:95], v[136:139], v[108:111]\n"
    "ds_read_b64_tr_b16 v[76:77], v8 offset:8192\n"
    "ds_read_b64_tr_b16 v[78:79], v8 offset:8704\n"
    "v_add_f32 v65, v58, v59\n"
    "v_add_f32 v66, v60, v61\n"
    "v_mfma_f32_16x16x32_bf16 v[112:115], v[92:95], v[140:143], v[112:115]\n"
    "ds_read_b128 v[144:147], v6 offset:16384\n"
    "ds_read_b128 v[148:151], v6 offset:18432\n"
    "s_add_u32 m0, s41, 0xc400\n"
    "s_nop 0\n"
    "buffer_load_dwordx4 v28, s[4:7], s2 offen lds\n"
    "v_add_f32 v67, v62, v63\n"
    "v_add_f32 v64, v64, v65\n"
    "v_mfma_f32_16x16x32_bf16 v[116:119], v[96:99], v[136:139], v[116:119]\n"
    "ds_read_b64_tr_b16 v[80:81], v35 offset:8192\n"
    "ds_read_b64_tr_b16 v[82:83], v35 offset:8704\n"
    "v_add_f32 v66, v66, v67\n"
    "v_add_f32 v64, v64, v66\n"
    "v_mfma_f32_16x16x32_bf16 v[120:123], v[96:99], v[140:143], v[120:123]\n"
    "s_bfe_u32 s2, s0, 0x40010\n"
    "s_lshl_b32 s2, s2, 7\n"
    "s_add_u32 s2, s2, s40\n"
    "s_mov_b32 m0, s41\n"
    "s_nop 0\n"
    "buffer_load_dwordx4 v2, s[4:7], s2 offen lds\n"
    "v_add_f32_dpp v64, v64, v64 row_shr:1 row_mask:0xf bank_mask:0xf bound_ctrl:0\n"
    "s_mul_i32 s38, s38, 0x9e3779b1\n"
    "v_mfma_f32_16x16x32_bf16 v[72:75], v[92:95], v[24:27], v[72:75]\n"
    "s_add_u32 s38, s38, 0x7f4a7c15\n"
    "v_add_f32_dpp v64, v64, v64 row_shr:2 row_mask:0xf bank_mask:0xf bound_ctrl:0\n"
    "s_waitcnt lgkmcnt(0)\n"
    "v_mfma_f32_16x16x32_bf16 v[108:111], v[76:79], v[144:147], v[108:111]\n"
    "ds_read_b64_tr_b16 v[92:93], v8 offset:12288\n"
    "ds_read_b64_tr_b16 v[94:95], v8 offset:12800\n"
    "v_mov_b32 v22, s38\n"
    "v_mov_b32 v69, s38\n"
    "v_mfma_f32_16x16x32_bf16 v[112:115], v[76:79], v[148:151], v[112:115]\n"
    "ds_read_b128 v[152:155], v29 offset:16384\n"
    "ds_read_b128 v[156:159], v29 offset:18432\n"
    "s_bfe_u32 s71, s0, 0x40010\n"
    "s_mul_i32 s71, s71, 0x30000\n"
    "s_add_u32 m0, s45, 0x1c000\n"
    "s_add_u32 s71, s71, s42\n"
    "buffer_load_dwordx4 v7, s[8:11], s71 offen lds\n"
    "v_add_f32_dpp v64, v64, v64 row_shr:4 row_mask:0xf bank_mask:0xf bound_ctrl:0\n"
    "v_mov_b32 v71, s38\n"
    "v_mfma_f32_16x16x32_bf16 v[116:119], v[80:83], v[144:147], v[116:119]\n"
    "ds_read_b64_tr_b16 v[96:97], v35 offset:12288\n"
    "ds_read_b64_tr_b16 v[98:99], v35 offset:12800\n"
    "v_mov_b32 v67, 0x3727c5ac\n"
    "v_add_f32_dpp v64, v64, v64 row_shr:8 row_mask:0xf bank_mask:0xf bound_ctrl:0\n"
    "v_mfma_f32_16x16x32_bf16 v[120:123], v[80:83], v[148:151], v[120:123]\n"
    "s_add_u32 m0, s41, 0x400\n"
    "s_nop 0\n"
    "buffer_load_dwordx4 v28, s[4:7], s2 offen lds\n"
    "v_add_f32_dpp v64, v64, v64 row_bcast:15 row_mask:0xa bank_mask:0xf\n"
    "s_nop 1\n"
    "v_add_f32_dpp v64, v64, v64 row_bcast:31 row_mask:0xc bank_mask:0xf\n"
    "v_mfma_f32_16x16x32_bf16 v[72:75], v[76:79], v[24:27], v[72:75]\n"
    "v_readlane_b32 s44, v64, 63\n"
    "s_nop 1\n"
    "v_mov_b32 v68, s44\n"
    "s_waitcnt vmcnt(6) lgkmcnt(0)\n"
    "s_barrier\n"
    "v_mfma_f32_16x16x32_bf16 v[108:111], v[92:95], v[152:155], v[108:111]\n"
    "ds_read_b64_tr_b16 v[76:77], v8 offset:16384\n"
    "ds_read_b64_tr_b16 v[78:79], v8 offset:16896\n"
    "v_mul_f32 v68, 0x3b000000, v68\n"
    "v_sub_f32 v56, v56, v68\n"
    "v_mfma_f32_16x16x32_bf16 v[112:115], v[92:95], v[156:159], v[112:115]\n"
    "ds_read_b128 v[160:163], v6 offset:32768\n"
    "ds_read_b128 v[164:167], v6 offset:34816\n"
    "s_bfe_u32 s2, s0, 0x40014\n"
    "s_lshl_b32 s2, s2, 7\n"
    "s_add_u32 s2, s2, s40\n"
    "s_add_u32 m0, s41, 0x4000\n"
    "s_nop 0\n"
    "buffer_load_dwordx4 v2, s[4:7], s2 offen lds\n"
    "v_sub_f32 v57, v57, v68\n"
    "v_sub_f32 v58, v58, v68\n"
    "v_mfma_f32_16x16x32_bf16 v[116:119], v[96:99], v[152:155], v[116:119]\n"
    "ds_read_b64_tr_b16 v[80:81], v35 offset:16384\n"
    "ds_read_b64_tr_b16 v[82:83], v35 offset:16896\n"
    "v_sub_f32 v59, v59, v68\n"
    "v_sub_f32 v60, v60, v68\n"
    "v_mfma_f32_16x16x32_bf16 v[120:123], v[96:99], v[156:159], v[120:123]\n"
    "s_bfe_u32 s71, s0, 0x40014\n"
    "s_mul_i32 s71, s71, 0x30000\n"
    "s_add_u32 m0, s45, 0x1e000\n"
    "s_add_u32 s71, s71, s42\n"
    "buffer_load_dwordx4 v7, s[8:11], s71 offen lds\n"
    "v_sub_f32 v61, v61, v68\n"
    "v_sub_f32 v62, v62, v68\n"
    "v_mfma_f32_16x16x32_bf16 v[72:75], v[92:95], v[24:27], v[72:75]\n"
    "v_sub_f32 v63, v63, v68\n"
    "v_mul_f32 v64, v56, v56\n"
    "s_waitcnt lgkmcnt(0)\n"
    "v_mfma_f32_16x16x32_bf16 v[108:111], v[76:79], v[160:163], v[108:111]\n"
    "ds_read_b64_tr_b16 v[92:93], v8 offset:20480\n"
    "ds_read_b64_tr_b16 v[94:95], v8 offset:20992\n"
    "v_mul_f32 v65, v57, v57\n"
    "v_fmac_f32 v64, v58, v58\n"
    "v_mfma_f32_16x16x32_bf16 v[112:115], v[76:79], v[164:167], v[112:115]\n"
    "ds_read_b128 v[168:171], v29 offset:32768\n"
    "ds_read_b128 v[172:175], v29 offset:34816\n"
    "s_add_u32 m0, s41, 0x4400\n"
    "s_nop 0\n"
    "buffer_load_dwordx4 v28, s[4:7], s2 offen lds\n"
    "v_fmac_f32 v65, v59, v59\n"
    "v_fmac_f32 v64, v60, v60\n"
    "v_mfma_f32_16x16x32_bf16 v[116:119], v[80:83], v[160:163], v[116:119]\n"
    "ds_read_b64_tr_b16 v[96:97], v35 offset:20480\n"
    "ds_read_b64_tr_b16 v[98:99], v35 offset:20992\n"
    "v_fmac_f32 v65, v61, v61\n"
    "v_fmac_f32 v64, v62, v62\n"
    "v_mfma_f32_16x16x32_bf16 v[120:123], v[80:83], v[164:167], v[120:123]\n"
    "s_mov_b64 exec, 0xff\n"
    "buffer_load_dword v124, v30, s[12:15], 0 offen\n"
    "buffer_load_dword v124, v30, s[16:19], 0 offen\n"
    "buffer_load_dword v124, v30, s[20:23], 0 offen\n"
    "s_mov_b64 exec, -1\n"
    "s_getpc_b64 s[60:61]\n"
    ".Lk_pc:\n"
    "s_mov_b32 s62, .Lk_end-.Lk_pc\n"
    "s_mov_b32 s63, 0x20000\n"
    "s_lshr_b32 s71, s32, 3\n"
    "s_lshl_b32 s71, s71, 3\n"
    "s_add_u32 s71, s71, s33\n"
    "s_lshl_b32 s71, s71, 7\n"
    "s_mov_b64 exec, 1\n"
    "buffer_load_dword v124, off, s[60:63], s71\n"
    "s_mov_b64 exec, -1\n"
    "v_fmac_f32 v65, v63, v63\n"
    "v_add_f32 v64, v64, v65\n"
    "v_mfma_f32_16x16x32_bf16 v[72:75], v[76:79], v[24:27], v[72:75]\n"
    "s_nop 0\n"
    "v_add_f32_dpp v64, v64, v64 row_shr:1 row_mask:0xf bank_mask:0xf bound_ctrl:0\n"
    "s_lshl_b32 s56, s56, 4\n"
    "s_waitcnt vmcnt(10) lgkmcnt(0)\n"
    "s_barrier\n"
    "v_mfma_f32_16x16x32_bf16 v[108:111], v[92:95], v[168:171], v[108:111]\n"
    "ds_read_b64_tr_b16 v[76:77], v8 offset:24576\n"
    "ds_read_b64_tr_b16 v[78:79], v8 offset:25088\n"
    "s_add_u32 s56, s56, 0x28000\n"
    "v_add_f32_dpp v64, v64, v64 row_shr:2 row_mask:0xf bank_mask:0xf bound_ctrl:0\n"
    "v_mfma_f32_16x16x32_bf16 v[112:115], v[92:95], v[172:175], v[112:115]\n"
    "ds_read_b128 v[176:179], v6 offset:49152\n"
    "ds_read_b128 v[180:183], v6 offset:51200\n"
    "s_bfe_u32 s2, s0, 0x40018\n"
    "s_lshl_b32 s2, s2, 7\n"
    "s_add_u32 s2, s2, s40\n"
    "s_add_u32 m0, s41, 0x8000\n"
    "s_nop 0\n"
    "buffer_load_dwordx4 v2, s[4:7], s2 offen lds\n"
    "v_add_f32_dpp v64, v64, v64 row_shr:4 row_mask:0xf bank_mask:0xf bound_ctrl:0\n"
    "s_nop 1\n"
    "v_add_f32_dpp v64, v64, v64 row_shr:8 row_mask:0xf bank_mask:0xf bound_ctrl:0\n"
    "v_mfma_f32_16x16x32_bf16 v[116:119], v[96:99], v[168:171], v[116:119]\n"
    "ds_read_b64_tr_b16 v[80:81], v35 offset:24576\n"
    "ds_read_b64_tr_b16 v[82:83], v35 offset:25088\n"
    "v_add_f32_dpp v64, v64, v64 row_bcast:15 row_mask:0xa bank_mask:0xf\n"
    "s_nop 1\n"
    "v_add_f32_dpp v64, v64, v64 row_bcast:31 row_mask:0xc bank_mask:0xf\n"
    "v_mfma_f32_16x16x32_bf16 v[120:123], v[96:99], v[172:175], v[120:123]\n"
    "s_bfe_u32 s71, s0, 0x40018\n"
    "s_mul_i32 s71, s71, 0x30000\n"
    "s_add_u32 m0, s45, 0x20000\n"
    "s_add_u32 s71, s71, s42\n"
    "buffer_load_dwordx4 v7, s[8:11], s71 offen lds\n"
    "v_readlane_b32 s44, v64, 63\n"
    "s_nop 1\n"
    "v_mov_b32 v70, s44\n"
    "v_mfma_f32_16x16x32_bf16 v[72:75], v[92:95], v[24:27], v[72:75]\n"
    "v_fmac_f32 v67, 0x3b000000, v70\n"
    "v_rsq_f32 v70, v67\n"
    "s_waitcnt lgkmcnt(0)\n"
    "v_mfma_f32_16x16x32_bf16 v[108:111], v[76:79], v[176:179], v[108:111]\n"
    "ds_read_b64_tr_b16 v[92:93], v8 offset:28672\n"
    "ds_read_b64_tr_b16 v[94:95], v8 offset:29184\n"
    "s_mov_b64 exec, 1\n"
    "buffer_store_dwordx4 v[68:71], off, s[28:31], s56 sc1\n"
    "s_mov_b64 exec, -1\n"
    "v_mfma_f32_16x16x32_bf16 v[112:115], v[76:79], v[180:183], v[112:115]\n"
    "ds_read_b128 v[184:187], v29 offset:49152\n"
    "ds_read_b128 v[188:191], v29 offset:51200\n"
    "s_add_u32 m0, s41, 0x8400\n"
    "s_nop 0\n"
    "buffer_load_dwordx4 v28, s[4:7], s2 offen lds\n"
    "v_mfma_f32_16x16x32_bf16 v[116:119], v[80:83], v[176:179], v[116:119]\n"
    "ds_read_b64_tr_b16 v[96:97], v35 offset:28672\n"
    "ds_read_b64_tr_b16 v[98:99], v35 offset:29184\n"
    "v_mfma_f32_16x16x32_bf16 v[120:123], v[80:83], v[180:183], v[120:123]\n"
    "v_mfma_f32_16x16x32_bf16 v[72:75], v[76:79], v[24:27], v[72:75]\n"
    "s_waitcnt vmcnt(11) lgkmcnt(0)\n"
    "s_barrier\n"
    "v_mfma_f32_16x16x32_bf16 v[108:111], v[92:95], v[184:187], v[108:111]\n"
    "v_add_u32 v8, 0x10000, v8\n"
    "ds_read_b64_tr_b16 v[76:77], v8 offset:49152\n"
    "v_mfma_f32_16x16x32_bf16 v[112:115], v[92:95], v[188:191], v[112:115]\n"
    "ds_read_b64_tr_b16 v[78:79], v8 offset:49664\n"
    "ds_read_b128 v[192:195], v6\n"
    "s_bfe_u32 s2, s0, 0x4001c\n"
    "s_lshl_b32 s2, s2, 7\n"
    "s_add_u32 s2, s2, s40\n"
    "s_add_u32 m0, s41, 0xc000\n"
    "s_nop 0\n"
    "buffer_load_dwordx4 v2, s[4:7], s2 offen lds\n"
    "v_mfma_f32_16x16x32_bf16 v[116:119], v[96:99], v[184:187], v[116:119]\n"
    "ds_read_b128 v[196:199], v6 offset:2048\n"
    "v_add_u32 v35, 0x10000, v35\n"
    "v_mfma_f32_16x16x32_bf16 v[120:123], v[96:99], v[188:191], v[120:123]\n"
    "ds_read_b64_tr_b16 v[80:81], v35 offset:49152\n"
    "ds_read_b64_tr_b16 v[82:83], v35 offset:49664\n"
    "s_bfe_u32 s71, s0, 0x4001c\n"
    "s_mul_i32 s71, s71, 0x30000\n"
    "s_add_u32 m0, s45, 0x22000\n"
    "s_add_u32 s71, s71, s42\n"
    "buffer_load_dwordx4 v7, s[8:11], s71 offen lds\n"
    "v_mfma_f32_16x16x32_bf16 v[72:75], v[92:95], v[24:27], v[72:75]\n"
    "s_waitcnt lgkmcnt(0)\n"
    "v_mfma_f32_16x16x32_bf16 v[108:111], v[76:79], v[192:195], v[108:111]\n"
    "ds_read_b64_tr_b16 v[92:93], v8 offset:53248\n"
    "ds_read_b64_tr_b16 v[94:95], v8 offset:53760\n"
    "v_mfma_f32_16x16x32_bf16 v[112:115], v[76:79], v[196:199], v[112:115]\n"
    "ds_read_b128 v[200:203], v29\n"
    "ds_read_b128 v[204:207], v29 offset:2048\n"
    "s_add_u32 m0, s41, 0xc400\n"
    "s_nop 0\n"
    "buffer_load_dwordx4 v28, s[4:7], s2 offen lds\n"
    "v_mfma_f32_16x16x32_bf16 v[116:119], v[80:83], v[192:195], v[116:119]\n"
    "ds_read_b64_tr_b16 v[96:97], v35 offset:53248\n"
    "ds_read_b64_tr_b16 v[98:99], v35 offset:53760\n"
    "v_mfma_f32_16x16x32_bf16 v[120:123], v[80:83], v[196:199], v[120:123]\n"
    "s_bfe_u32 s71, s1, 0x40000\n"
    "s_mul_i32 s71, s71, 0x30000\n"
    "s_add_u32 m0, s45, 0x8000\n"
    "s_add_u32 s71, s71, s43\n"
    "buffer_load_dwordx4 v3, s[8:11], s71 offen lds\n"
    "v_mfma_f32_16x16x32_bf16 v[72:75], v[76:79], v[24:27], v[72:75]\n"
    "s_waitcnt vmcnt(12) lgkmcnt(0)\n"
    "s_barrier\n"
    "v_mfma_f32_16x16x32_bf16 v[108:111], v[92:95], v[200:203], v[108:111]\n"
    "ds_read_b64_tr_b16 v[76:77], v8 offset:57344\n"
    "ds_read_b64_tr_b16 v[78:79], v8 offset:57856\n"
    "s_mul_i32 s66, s39, 0xc00\n"
    "s_sub_u32 s2, s52, s66\n"
    "v_mfma_f32_16x16x32_bf16 v[112:115], v[92:95], v[204:207], v[112:115]\n"
    "ds_read_b128 v[208:211], v6 offset:16384\n"
    "ds_read_b128 v[212:215], v6 offset:18432\n"
    "s_bfe_u32 s71, s0, 0x40000\n"
    "s_mul_i32 s71, s71, 0x38000\n"
    "s_add_u32 m0, s45, 0xa000\n"
    "s_add_u32 s71, s71, s46\n"
    "buffer_load_dwordx4 v4, s[12:15], s71 offen lds\n"
    "s_cmp_eq_u32 s39, 1\n"
    "s_cselect_b64 s[72:73], -1, 0\n"
    "s_and_b32 s66, s36, 1\n"
    "v_mfma_f32_16x16x32_bf16 v[116:119], v[96:99], v[200:203], v[116:119]\n"
    "ds_read_b64_tr_b16 v[80:81], v35 offset:57344\n"
    "ds_read_b64_tr_b16 v[82:83], v35 offset:57856\n"
    "s_lshl_b32 s67, s66, 12\n"
    "s_add_u32 s2, s2, s67\n"
    "v_mfma_f32_16x16x32_bf16 v[120:123], v[96:99], v[204:207], v[120:123]\n"
    "s_lshl_b32 s67, s67, 1\n"
    "s_sub_u32 s56, 0x1000, s67\n"
    "v_mfma_f32_16x16x32_bf16 v[72:75], v[92:95], v[24:27], v[72:75]\n"
    "s_lshl_b32 s67, s66, 7\n"
    "v_add_u32 v56, s67, v12\n"
    "s_waitcnt lgkmcnt(0)\n"
    "v_mfma_f32_16x16x32_bf16 v[108:111], v[76:79], v[208:211], v[108:111]\n"
    "ds_read_b64_tr_b16 v[92:93], v8 offset:61440\n"
    "ds_read_b64_tr_b16 v[94:95], v8 offset:61952\n"
    "v_xor_b32 v57, 0x80, v56\n"
    "s_lshr_b32 s67, s36, 1\n"
    "v_mfma_f32_16x16x32_bf16 v[112:115], v[76:79], v[212:215], v[112:115]\n"
    "ds_read_b128 v[216:219], v29 offset:16384\n"
    "ds_read_b128 v[220:223], v29 offset:18432\n"
    "s_bfe_u32 s71, s0, 0x40004\n"
    "s_mul_i32 s71, s71, 0x38000\n"
    "s_mov_b32 m0, s45\n"
    "s_add_u32 s71, s71, s46\n"
    "buffer_load_dwordx4 v4, s[12:15], s71 offen lds\n"
    "s_lshl_b32 s67, s67, 8\n"
    "v_add_u32 v58, s67, v56\n"
    "v_mfma_f32_16x16x32_bf16 v[116:119], v[80:83], v[208:211], v[116:119]\n"
    "ds_read_b64_tr_b16 v[96:97], v35 offset:61440\n"
    "ds_read_b64_tr_b16 v[98:99], v35 offset:61952\n"
    "s_xor_b32 s66, s66, s39\n"
    "s_cmp_eq_u32 s66, 1\n"
    "s_cselect_b64 s[62:63], -1, 0\n"
    "v_mfma_f32_16x16x32_bf16 v[120:123], v[80:83], v[212:215], v[120:123]\n"
    "v_mfma_f32_16x16x32_bf16 v[72:75], v[76:79], v[24:27], v[72:75]\n"
    "s_waitcnt vmcnt(6) lgkmcnt(0)\n"
    "s_barrier\n"
    "v_mfma_f32_16x16x32_bf16 v[108:111], v[92:95], v[216:219], v[108:111]\n"
    "v_add_u32 v8, 0x10000, v8\n"
    "ds_read_b64_tr_b16 v[76:77], v8\n"
    "v_mfma_f32_16x16x32_bf16 v[112:115], v[92:95], v[220:223], v[112:115]\n"
    "ds_read_b64_tr_b16 v[78:79], v8 offset:512\n"
    "ds_read_b128 v[224:227], v6 offset:32768\n"
    "s_bfe_u32 s71, s1, 0x40008\n"
    "s_mul_i32 s71, s71, 0x30000\n"
    "s_add_u32 m0, s45, 0x2000\n"
    "s_add_u32 s71, s71, s43\n"
    "buffer_load_dwordx4 v3, s[8:11], s71 offen lds\n"
    "v_mfma_f32_16x16x32_bf16 v[116:119], v[96:99], v[216:219], v[116:119]\n"
    "ds_read_b128 v[228:231], v6 offset:34816\n"
    "v_add_u32 v35, 0x10000, v35\n"
    "v_mfma_f32_16x16x32_bf16 v[120:123], v[96:99], v[220:223], v[120:123]\n"
    "ds_read_b64_tr_b16 v[80:81], v35\n"
    "ds_read_b64_tr_b16 v[82:83], v35 offset:512\n"
    "v_mfma_f32_16x16x32_bf16 v[72:75], v[92:95], v[24:27], v[72:75]\n"
    "s_waitcnt lgkmcnt(0)\n"
    "v_mfma_f32_16x16x32_bf16 v[108:111], v[76:79], v[224:227], v[108:111]\n"
    "ds_read_b64_tr_b16 v[92:93], v8 offset:4096\n"
    "ds_read_b64_tr_b16 v[94:95], v8 offset:4608\n"
    "v_mfma_f32_16x16x32_bf16 v[112:115], v[76:79], v[228:231], v[112:115]\n"
    "ds_read_b128 v[232:235], v29 offset:32768\n"
    "ds_read_b128 v[236:239], v29 offset:34816\n"
    "v_and_b32 v23, 0xf0, v1\n"
    "buffer_load_dwordx4 v[40:43], v23, s[28:31], s55 offen sc1\n"
    "buffer_load_dwordx4 v[44:47], v23, s[28:31], s55 offen offset:256 sc1\n"
    "v_mfma_f32_16x16x32_bf16 v[116:119], v[80:83], v[224:227], v[116:119]\n"
    "ds_read_b64_tr_b16 v[96:97], v35 offset:4096\n"
    "ds_read_b64_tr_b16 v[98:99], v35 offset:4608\n"
    "v_mfma_f32_16x16x32_bf16 v[120:123], v[80:83], v[228:231], v[120:123]\n"
    "v_mfma_f32_16x16x32_bf16 v[72:75], v[76:79], v[24:27], v[72:75]\n"
    "s_waitcnt vmcnt(6) lgkmcnt(0)\n"
    "s_barrier\n"
    "v_mfma_f32_16x16x32_bf16 v[108:111], v[92:95], v[232:235], v[108:111]\n"
    "ds_read_b64_tr_b16 v[76:77], v8 offset:8192\n"
    "ds_read_b64_tr_b16 v[78:79], v8 offset:8704\n"
    "v_mfma_f32_16x16x32_bf16 v[112:115], v[92:95], v[236:239], v[112:115]\n"
    "ds_read_b128 v[240:243], v6 offset:49152\n"
    "ds_read_b128 v[244:247], v6 offset:51200\n"
    "s_bfe_u32 s71, s0, 0x40008\n"
    "s_mul_i32 s71, s71, 0x38000\n"
    "s_add_u32 m0, s45, 0x4000\n"
    "s_add_u32 s71, s71, s46\n"
    "buffer_load_dwordx4 v4, s[12:15], s71 offen lds\n"
    "v_mfma_f32_16x16x32_bf16 v[116:119], v[96:99], v[232:235], v[116:119]\n"
    "ds_read_b64_tr_b16 v[80:81], v35 offset:8192\n"
    "ds_read_b64_tr_b16 v[82:83], v35 offset:8704\n"
    "v_mfma_f32_16x16x32_bf16 v[120:123], v[96:99], v[236:239], v[120:123]\n"
    "v_mfma_f32_16x16x32_bf16 v[72:75], v[92:95], v[24:27], v[72:75]\n"
    "s_waitcnt lgkmcnt(0)\n"
    "v_mfma_f32_16x16x32_bf16 v[108:111], v[76:79], v[240:243], v[108:111]\n"
    "ds_read_b64_tr_b16 v[92:93], v8 offset:12288\n"
    "ds_read_b64_tr_b16 v[94:95], v8 offset:12800\n"
    "v_mfma_f32_16x16x32_bf16 v[112:115], v[76:79], v[244:247], v[112:115]\n"
    "ds_read_b128 v[248:251], v29 offset:49152\n"
    "ds_read_b128 v[252:255], v29 offset:51200\n"
    "s_bfe_u32 s71, s0, 0x4000c\n"
    "s_mul_i32 s71, s71, 0x38000\n"
    "s_add_u32 m0, s45, 0x6000\n"
    "s_add_u32 s71, s71, s46\n"
    "buffer_load_dwordx4 v4, s[12:15], s71 offen lds\n"
    "v_mfma_f32_16x16x32_bf16 v[116:119], v[80:83], v[240:243], v[116:119]\n"
    "ds_read_b64_tr_b16 v[96:97], v35 offset:12288\n"
    "ds_read_b64_tr_b16 v[98:99], v35 offset:12800\n"
    "v_mfma_f32_16x16x32_bf16 v[120:123], v[80:83], v[244:247], v[120:123]\n"
    "v_mfma_f32_16x16x32_bf16 v[72:75], v[76:79], v[24:27], v[72:75]\n"
    "s_waitcnt lgkmcnt(0)\n"
    "v_mfma_f32_16x16x32_bf16 v[72:75], v[92:95], v[24:27], v[72:75]\n"
    "v_mfma_f32_16x16x32_bf16 v[108:111], v[92:95], v[248:251], v[108:111]\n"
    "v_mfma_f32_16x16x32_bf16 v[112:115], v[92:95], v[252:255], v[112:115]\n"
    "v_mfma_f32_16x16x32_bf16 v[116:119], v[96:99], v[248:251], v[116:119]\n"
    "v_mfma_f32_16x16x32_bf16 v[120:123], v[96:99], v[252:255], v[120:123]\n"
    "s_mov_b32 exec_lo, 0x10001\n"
    "s_mov_b32 exec_hi, 0x10001\n"
    "s_nop 1\n"
    "ds_write_b128 v58, v[72:75]\n"
    "s_mov_b64 exec, -1\n"
    "s_waitcnt vmcnt(2)\n"
    "v_xor_b32 v23, v22, v41\n"
    "v_xor_b32 v124, v22, v43\n"
    "v_or_b32 v23, v23, v124\n"
    "v_xor_b32 v124, v22, v45\n"
    "v_or_b32 v23, v23, v124\n"
    "v_xor_b32 v124, v22, v47\n"
    "v_or_b32 v23, v23, v124\n"
    "v_cmp_ne_u32 vcc, 0, v23\n"
    "s_cbranch_vccnz .Lk_spin_1\n"
    ".Lk_ok_2:\n"
    "v_mul_f32 v40, -v40, v42\n"
    "v_mul_f32 v44, -v44, v46\n"
    "s_waitcnt lgkmcnt(0)\n"
    "s_barrier\n"
    "; ---- passes 2, 3\n"
    "s_cmp_eq_u32 s37, 1\n"
    "s_cbranch_scc1 .Lk_w1_ch1\n"
    "ds_read_b128 v[36:39], v56\n"
    "ds_read_b128 v[124:127], v57\n"
    "ds_read_b64_tr_b16 v[76:77], v9 offset:32768\n"
    "ds_read_b64_tr_b16 v[78:79], v9 offset:33024\n"
    "ds_read_b64_tr_b16 v[80:81], v14 offset:40960\n"
    "ds_read_b64_tr_b16 v[82:83], v14 offset:41472\n"
    "ds_read_b64_tr_b16 v[84:85], v15 offset:40960\n"
    "ds_read_b64_tr_b16 v[86:87], v15 offset:41472\n"
    "ds_read_b64_tr_b16 v[88:89], v18 offset:32768\n"
    "ds_read_b64_tr_b16 v[90:91], v18 offset:33024\n"
    "s_bfe_u32 s71, s1, 0x40010\n"
    "s_mul_i32 s71, s71, 0x30000\n"
    "s_add_u32 m0, s45, 0xc000\n"
    "s_add_u32 s71, s71, s43\n"
    "buffer_load_dwordx4 v3, s[8:11], s71 offen lds\n"
    "s_bfe_u32 s71, s0, 0x40010\n"
    "s_mul_i32 s71, s71, 0x38000\n"
    "s_add_u32 m0, s45, 0xe000\n"
    "s_add_u32 s71, s71, s46\n"
    "buffer_load_dwordx4 v4, s[12:15], s71 offen lds\n"
    "s_waitcnt lgkmcnt(9)\n"
    "v_pk_mul_f32 v[108:109], v[108:109], v[42:43] op_sel_hi:[1,0]\n"
    "v_pk_mul_f32 v[110:111], v[110:111], v[42:43] op_sel_hi:[1,0]\n"
    "v_pk_fma_f32 v[108:109], v[36:37], v[40:41], v[108:109] op_sel_hi:[1,0,1]\n"
    "v_pk_fma_f32 v[110:111], v[38:39], v[40:41], v[110:111] op_sel_hi:[1,0,1]\n"
    "v_pk_mul_f32 v[112:113], v[112:113], v[46:47] op_sel_hi:[1,0]\n"
    "v_pk_mul_f32 v[114:115], v[114:115], v[46:47] op_sel_hi:[1,0]\n"
    "v_pk_fma_f32 v[112:113], v[36:37], v[44:45], v[112:113] op_sel_hi:[1,0,1]\n"
    "v_pk_fma_f32 v[114:115], v[38:39], v[44:45], v[114:115] op_sel_hi:[1,0,1]\n"
    "v_cvt_pk_bf16_f32 v108, v108, v109\n"
    "v_cvt_pk_bf16_f32 v109, v110, v111\n"
    "v_cvt_pk_bf16_f32 v110, v112, v113\n"
    "v_cvt_pk_bf16_f32 v111, v114, v115\n"
    "s_nop 0\n"
    "v_permlane16_swap_b32 v108, v110\n"
    "v_permlane16_swap_b32 v109, v111\n"
    "buffer_store_dwordx4 v[108:111], v1, s[28:31], s2 offen sc1\n"
    "s_add_u32 s2, s2, s56\n"
    "s_waitcnt lgkmcnt(8)\n"
    "v_pk_mul_f32 v[116:117], v[116:117], v[42:43] op_sel_hi:[1,0]\n"
    "v_pk_mul_f32 v[118:119], v[118:119], v[42:43] op_sel_hi:[1,0]\n"
    "v_pk_fma_f32 v[116:117], v[124:125], v[40:41], v[116:117] op_sel_hi:[1,0,1]\n"
    "v_pk_fma_f32 v[118:119], v[126:127], v[40:41], v[118:119] op_sel_hi:[1,0,1]\n"
    "v_pk_mul_f32 v[120:121], v[120:121], v[46:47] op_sel_hi:[1,0]\n"
    "v_pk_mul_f32 v[122:123], v[122:123], v[46:47] op_sel_hi:[1,0]\n"
    "v_pk_fma_f32 v[120:121], v[124:125], v[44:45], v[120:121] op_sel_hi:[1,0,1]\n"
    "v_pk_fma_f32 v[122:123], v[126:127], v[44:45], v[122:123] op_sel_hi:[1,0,1]\n"
    "v_cvt_pk_bf16_f32 v116, v116, v117\n"
    "v_cvt_pk_bf16_f32 v117, v118, v119\n"
    "v_cvt_pk_bf16_f32 v118, v120, v121\n"
    "v_cvt_pk_bf16_f32 v119, v122, v123\n"
    "s_nop 0\n"
    "v_permlane16_swap_b32 v116, v118\n"
    "v_permlane16_swap_b32 v117, v119\n"
    "buffer_store_dwordx4 v[116:119], v1, s[28:31], s2 offen sc1\n"
    "v_cndmask_b32 v30, v21, v20, s[72:73]\n"
    "v_cndmask_b32 v21, v20, v21, s[72:73]\n"
    "v_cndmask_b32 v32, v108, v116, s[62:63]\n"
    "v_cndmask_b32 v33, v109, v117, s[62:63]\n"
    "v_cndmask_b32 v34, v110, v118, s[62:63]\n"
    "v_cndmask_b32 v35, v111, v119, s[62:63]\n"
    "ds_write_b128 v30, v[32:35] offset:32768\n"
    "s_waitcnt lgkmcnt(1)\n"
    "v_mfma_f32_16x16x32_bf16 v[48:51], v[76:79], v[128:131], 0\n"
    "ds_read_b64_tr_b16 v[92:93], v9 offset:34816\n"
    "ds_read_b64_tr_b16 v[94:95], v9 offset:35072\n"
    "s_bfe_u32 s71, s0, 0x40014\n"
    "v_mfma_f32_16x16x32_bf16 v[52:55], v[76:79], v[132:135], 0\n"
    "ds_read_b64_tr_b16 v[96:97], v14 offset:45056\n"
    "ds_read_b64_tr_b16 v[98:99], v14 offset:45568\n"
    "s_mul_i32 s71, s71, 0x38000\n"
    "v_mfma_f32_16x16x32_bf16 v[56:59], v[80:83], v[128:131], 0\n"
    "ds_read_b64_tr_b16 v[100:101], v15 offset:45056\n"
    "ds_read_b64_tr_b16 v[102:103], v15 offset:45568\n"
    "s_add_u32 m0, s45, 0x10000\n"
    "v_mfma_f32_16x16x32_bf16 v[60:63], v[80:83], v[132:135], 0\n"
    "ds_read_b64_tr_b16 v[104:105], v18 offset:34816\n"
    "ds_read_b64_tr_b16 v[106:107], v18 offset:35072\n"
    "s_add_u32 s71, s71, s46\n"
    "v_mfma_f32_16x16x32_bf16 v[64:67], v[84:87], v[128:131], 0\n"
    "buffer_load_dwordx4 v4, s[12:15], s71 offen lds\n"
    "s_cmp_ge_u32 s34, 2\n"
    "s_cselect_b32 s66, 0x80000, 0\n"
    "s_cselect_b32 s67, 0, 0x40000000\n"
    "s_cselect_b32 s68, 0x1000, 0\n"
    "s_sub_u32 s53, s52, s66\n"
    "v_mfma_f32_16x16x32_bf16 v[68:71], v[84:87], v[132:135], 0\n"
    "v_add_u32 v126, s67, v1\n"
    "s_cmp_ge_u32 s34, 1\n"
    "s_cselect_b32 s66, 0x40000, 0\n"
    "s_cselect_b32 s67, 0, 0x40000000\n"
    "s_cselect_b32 s69, 0x800, 0\n"
    "s_sub_u32 s54, s52, s66\n"
    "v_mfma_f32_16x16x32_bf16 v[72:75], v[88:91], v[24:27], 0\n"
    "v_add_u32 v127, s67, v1\n"
    "s_lshl_b32 s66, s39, 3\n"
    "s_waitcnt lgkmcnt(0)\n"
    "v_mfma_f32_16x16x32_bf16 v[48:51], v[92:95], v[136:139], v[48:51]\n"
    "ds_read_b64_tr_b16 v[76:77], v9 offset:36864\n"
    "ds_read_b64_tr_b16 v[78:79], v9 offset:37120\n"
    "s_bfe_u32 s71, s1, 0x40018\n"
    "v_mfma_f32_16x16x32_bf16 v[52:55], v[92:95], v[140:143], v[52:55]\n"
    "ds_read_b64_tr_b16 v[80:81], v14\n"
    "ds_read_b64_tr_b16 v[82:83], v14 offset:512\n"
    "s_mul_i32 s71, s71, 0x30000\n"
    "v_mfma_f32_16x16x32_bf16 v[56:59], v[96:99], v[136:139], v[56:59]\n"
    "ds_read_b64_tr_b16 v[84:85], v15\n"
    "ds_read_b64_tr_b16 v[86:87], v15 offset:512\n"
    "s_add_u32 m0, s45, 0x12000\n"
    "v_mfma_f32_16x16x32_bf16 v[60:63], v[96:99], v[140:143], v[60:63]\n"
    "ds_read_b64_tr_b16 v[88:89], v18 offset:36864\n"
    "ds_read_b64_tr_b16 v[90:91], v18 offset:37120\n"
    "s_add_u32 s71, s71, s43\n"
    "v_mfma_f32_16x16x32_bf16 v[64:67], v[100:103], v[136:139], v[64:67]\n"
    "buffer_load_dwordx4 v3, s[8:11], s71 offen lds\n"
    "s_sub_u32 s66, s49, s66\n"
    "v_and_b32 v37, 1, v0\n"
    "v_mfma_f32_16x16x32_bf16 v[68:71], v[100:103], v[140:143], v[68:71]\n"
    "v_lshl_add_u32 v125, v37, 3, s66\n"
    "s_mov_b64 exec, 3\n"
    "v_subrev_u32 v125, s68, v125\n"
    "s_mov_b64 exec, 12\n"
    "v_subrev_u32 v125, s69, v125\n"
    "s_mov_b64 exec, -1\n"
    "s_lshl_b32 s66, s39, 10\n"
    "v_mfma_f32_16x16x32_bf16 v[72:75], v[104:107], v[24:27], v[72:75]\n"
    "s_sub_u32 s42, s52, s66\n"
    "s_add_u32 s42, s42, 0x400\n"
    "s_waitcnt lgkmcnt(0)\n"
    "v_mfma_f32_16x16x32_bf16 v[48:51], v[76:79], v[144:147], v[48:51]\n"
    "ds_read_b64_tr_b16 v[92:93], v9 offset:38912\n"
    "ds_read_b64_tr_b16 v[94:95], v9 offset:39168\n"
    "s_bfe_u32 s71, s0, 0x40018\n"
    "v_mfma_f32_16x16x32_bf16 v[52:55], v[76:79], v[148:151], v[52:55]\n"
    "ds_read_b64_tr_b16 v[96:97], v14 offset:4096\n"
    "ds_read_b64_tr_b16 v[98:99], v14 offset:4608\n"
    "s_mul_i32 s71, s71, 0x38000\n"
    "v_mfma_f32_16x16x32_bf16 v[56:59], v[80:83], v[144:147], v[56:59]\n"
    "ds_read_b64_tr_b16 v[100:101], v15 offset:4096\n"
    "ds_read_b64_tr_b16 v[102:103], v15 offset:4608\n"
    "s_add_u32 m0, s45, 0x14000\n"
    "v_mfma_f32_16x16x32_bf16 v[60:63], v[80:83], v[148:151], v[60:63]\n"
    "ds_read_b64_tr_b16 v[104:105], v18 offset:38912\n"
    "ds_read_b64_tr_b16 v[106:107], v18 offset:39168\n"
    "s_add_u32 s71, s71, s46\n"
    "v_mfma_f32_16x16x32_bf16 v[64:67], v[84:87], v[144:147], v[64:67]\n"
    "buffer_load_dwordx4 v4, s[12:15], s71 offen lds\n"
    "s_min_u32 s65, s47, 8\n"
    "v_and_b32 v2, 7, v0\n"
    "v_mfma_f32_16x16x32_bf16 v[68:71], v[84:87], v[148:151], v[68:71]\n"
    "v_lshlrev_b32 v2, 2, v2\n"
    "s_and_b32 s66, s32, 7\n"
    "s_lshl_b32 s66, s66, 2\n"
    "v_mfma_f32_16x16x32_bf16 v[72:75], v[88:91], v[24:27], v[72:75]\n"
    "s_lshr_b32 s67, s32, 6\n"
    "s_add_u32 s66, s66, s67\n"
    "s_waitcnt vmcnt(7) lgkmcnt(0)\n"
    "s_barrier\n"
    "v_mfma_f32_16x16x32_bf16 v[48:51], v[92:95], v[152:155], v[48:51]\n"
    "ds_read_b64_tr_b16 v[76:77], v9 offset:8192\n"
    "ds_read_b64_tr_b16 v[78:79], v9 offset:8448\n"
    "s_waitcnt vmcnt(3)\n"
    "s_add_u32 s71, s49, 0x8000\n"
    "s_mov_b64 exec, 1\n"
    "buffer_store_dword v22, off, s[28:31], s71 sc1\n"
    "s_mov_b64 exec, -1\n"
    "v_mfma_f32_16x16x32_bf16 v[52:55], v[92:95], v[156:159], v[52:55]\n"
    "ds_read_b64_tr_b16 v[80:81], v14 offset:16384\n"
    "ds_read_b64_tr_b16 v[82:83], v14 offset:16896\n"
    "s_bfe_u32 s71, s0, 0x4001c\n"
    "v_mfma_f32_16x16x32_bf16 v[56:59], v[96:99], v[152:155], v[56:59]\n"
    "ds_read_b64_tr_b16 v[84:85], v15 offset:16384\n"
    "ds_read_b64_tr_b16 v[86:87], v15 offset:16896\n"
    "s_mul_i32 s71, s71, 0x38000\n"
    "v_mfma_f32_16x16x32_bf16 v[60:63], v[96:99], v[156:159], v[60:63]\n"
    "ds_read_b64_tr_b16 v[88:89], v18 offset:8192\n"
    "ds_read_b64_tr_b16 v[90:91], v18 offset:8448\n"
    "s_add_u32 m0, s45, 0x16000\n"
    "v_mfma_f32_16x16x32_bf16 v[64:67], v[100:103], v[152:155], v[64:67]\n"
    "s_add_u32 s71, s71, s46\n"
    "buffer_load_dwordx4 v4, s[12:15], s71 offen lds\n"
    "s_bfe_u32 s67, s32, 0x30003\n"
    "v_mfma_f32_16x16x32_bf16 v[68:71], v[100:103], v[156:159], v[68:71]\n"
    "s_lshl_b32 s67, s67, 7\n"
    "s_lshl_b32 s69, s37, 13\n"
    "s_add_u32 s69, s69, s67\n"
    "v_mfma_f32_16x16x32_bf16 v[72:75], v[104:107], v[24:27], v[72:75]\n"
    "s_mul_i32 s59, s36, 0x70000\n"
    "s_add_u32 s59, s59, s69\n"
    "s_waitcnt lgkmcnt(0)\n"
    "v_mfma_f32_16x16x32_bf16 v[48:51], v[76:79], v[160:163], v[48:51]\n"
    "ds_read_b64_tr_b16 v[92:93], v9 offset:10240\n"
    "ds_read_b64_tr_b16 v[94:95], v9 offset:10496\n"
    "s_bfe_u32 s71, s0, 0x40000\n"
    "v_mfma_f32_16x16x32_bf16 v[52:55], v[76:79], v[164:167], v[52:55]\n"
    "ds_read_b64_tr_b16 v[96:97], v14 offset:20480\n"
    "ds_read_b64_tr_b16 v[98:99], v14 offset:20992\n"
    "s_mul_i32 s71, s71, 0x38000\n"
    "v_mfma_f32_16x16x32_bf16 v[56:59], v[80:83], v[160:163], v[56:59]\n"
    "ds_read_b64_tr_b16 v[100:101], v15 offset:20480\n"
    "ds_read_b64_tr_b16 v[102:103], v15 offset:20992\n"
    "s_add_u32 m0, s45, 0x18000\n"
    "v_mfma_f32_16x16x32_bf16 v[60:63], v[80:83], v[164:167], v[60:63]\n"
    "ds_read_b64_tr_b16 v[104:105], v18 offset:10240\n"
    "ds_read_b64_tr_b16 v[106:107], v18 offset:10496\n"
    "s_add_u32 s71, s71, s3\n"
    "v_mfma_f32_16x16x32_bf16 v[64:67], v[84:87], v[160:163], v[64:67]\n"
    "buffer_load_dwordx4 v10, s[12:15], s71 offen lds\n"
    "s_lshl_b32 s60, s36, 17\n"
    "s_add_u32 s60, s60, s69\n"
    "v_mfma_f32_16x16x32_bf16 v[68:71], v[84:87], v[164:167], v[68:71]\n"
    "s_lshl_b32 s61, s36, 11\n"
    "s_lshl_b32 s69, s37, 10\n"
    "s_add_u32 s61, s61, s69\n"
    "v_mfma_f32_16x16x32_bf16 v[72:75], v[88:91], v[24:27], v[72:75]\n"
    "s_lshl_b32 s68, s66, 1\n"
    "s_add_u32 s68, s68, s37\n"
    "s_waitcnt lgkmcnt(0)\n"
    "v_mfma_f32_16x16x32_bf16 v[48:51], v[92:95], v[168:171], v[48:51]\n"
    "ds_read_b64_tr_b16 v[76:77], v9 offset:12288\n"
    "ds_read_b64_tr_b16 v[78:79], v9 offset:12544\n"
    "s_bfe_u32 s71, s0, 0x40004\n"
    "v_mfma_f32_16x16x32_bf16 v[52:55], v[92:95], v[172:175], v[52:55]\n"
    "ds_read_b64_tr_b16 v[80:81], v14 offset:24576\n"
    "ds_read_b64_tr_b16 v[82:83], v14 offset:25088\n"
    "s_mul_i32 s71, s71, 0x38000\n"
    "v_mfma_f32_16x16x32_bf16 v[56:59], v[96:99], v[168:171], v[56:59]\n"
    "ds_read_b64_tr_b16 v[84:85], v15 offset:24576\n"
    "ds_read_b64_tr_b16 v[86:87], v15 offset:25088\n"
    "s_add_u32 m0, s45, 0x1a000\n"
    "v_mfma_f32_16x16x32_bf16 v[60:63], v[96:99], v[172:175], v[60:63]\n"
    "ds_read_b64_tr_b16 v[88:89], v18 offset:12288\n"
    "ds_read_b64_tr_b16 v[90:91], v18 offset:12544\n"
    "s_add_u32 s71, s71, s3\n"
    "v_mfma_f32_16x16x32_bf16 v[64:67], v[100:103], v[168:171], v[64:67]\n"
    "buffer_load_dwordx4 v10, s[12:15], s71 offen lds\n"
    "s_mul_i32 s58, s68, 0x90\n"
    "s_mul_i32 s69, s36, 28\n"
    "v_mfma_f32_16x16x32_bf16 v[68:71], v[100:103], v[172:175], v[68:71]\n"
    "s_add_u32 s58, s58, s69\n"
    "s_lshl_b32 s58, s58, 10\n"
    "s_add_u32 s58, s58, 0x600000\n"
    "v_mfma_f32_16x16x32_bf16 v[72:75], v[104:107], v[24:27], v[72:75]\n"
    "s_lshl_b32 s62, s68, 2\n"
    "s_add_u32 s62, s62, s36\n"
    "s_waitcnt lgkmcnt(0)\n"
    "v_mfma_f32_16x16x32_bf16 v[48:51], v[76:79], v[176:179], v[48:51]\n"
    "ds_read_b64_tr_b16 v[92:93], v9 offset:14336\n"
    "ds_read_b64_tr_b16 v[94:95], v9 offset:14592\n"
    "s_bfe_u32 s71, s0, 0x40008\n"
    "v_mfma_f32_16x16x32_bf16 v[52:55], v[76:79], v[180:183], v[52:55]\n"
    "ds_read_b64_tr_b16 v[96:97], v14 offset:28672\n"
    "ds_read_b64_tr_b16 v[98:99], v14 offset:29184\n"
    "s_mul_i32 s71, s71, 0x38000\n"
    "v_mfma_f32_16x16x32_bf16 v[56:59], v[80:83], v[176:179], v[56:59]\n"
    "ds_read_b64_tr_b16 v[100:101], v15 offset:28672\n"
    "ds_read_b64_tr_b16 v[102:103], v15 offset:29184\n"
    "s_add_u32 m0, s45, 0x8000\n"
    "v_mfma_f32_16x16x32_bf16 v[60:63], v[80:83], v[180:183], v[60:63]\n"
    "ds_read_b64_tr_b16 v[104:105], v18 offset:14336\n"
    "ds_read_b64_tr_b16 v[106:107], v18 offset:14592\n"
    "s_add_u32 s71, s71, s3\n"
    "v_mfma_f32_16x16x32_bf16 v[64:67], v[84:87], v[176:179], v[64:67]\n"
    "buffer_load_dwordx4 v10, s[12:15], s71 offen lds\n"
    "s_lshl_b32 s62, s62, 7\n"
    "s_lshl_b32 s63, s66, 3\n"
    "v_mfma_f32_16x16x32_bf16 v[68:71], v[84:87], v[180:183], v[68:71]\n"
    "s_add_u32 s63, s63, s33\n"
    "s_lshl_b32 s63, s63, 13\n"
    "s_add_u32 s63, s63, s67\n"
    "v_mfma_f32_16x16x32_bf16 v[72:75], v[88:91], v[24:27], v[72:75]\n"
    "v_bfe_u32 v37, v0, 2, 2\n"
    "v_lshrrev_b32 v38, 5, v0\n"
    "s_waitcnt vmcnt(7) lgkmcnt(0)\n"
    "s_barrier\n"
    "v_mfma_f32_16x16x32_bf16 v[48:51], v[92:95], v[184:187], v[48:51]\n"
    "ds_read_b64_tr_b16 v[76:77], v9 offset:49152\n"
    "ds_read_b64_tr_b16 v[78:79], v9 offset:49408\n"
    "s_bfe_u32 s71, s0, 0x4000c\n"
    "v_mfma_f32_16x16x32_bf16 v[52:55], v[92:95], v[188:191], v[52:55]\n"
    "ds_read_b64_tr_b16 v[80:81], v14 offset:57344\n"
    "ds_read_b64_tr_b16 v[82:83], v14 offset:57856\n"
    "s_mul_i32 s71, s71, 0x38000\n"
    "v_mfma_f32_16x16x32_bf16 v[56:59], v[96:99], v[184:187], v[56:59]\n"
    "ds_read_b64_tr_b16 v[84:85], v15 offset:57344\n"
    "ds_read_b64_tr_b16 v[86:87], v15 offset:57856\n"
    "s_add_u32 m0, s45, 0xa000\n"
    "v_mfma_f32_16x16x32_bf16 v[60:63], v[96:99], v[188:191], v[60:63]\n"
    "ds_read_b64_tr_b16 v[88:89], v18 offset:49152\n"
    "ds_read_b64_tr_b16 v[90:91], v18 offset:49408\n"
    "s_add_u32 s71, s71, s3\n"
    "v_mfma_f32_16x16x32_bf16 v[64:67], v[100:103], v[184:187], v[64:67]\n"
    "buffer_load_dwordx4 v10, s[12:15], s71 offen lds\n"
    "v_lshl_add_u32 v38, v38, 3, v37\n"
    "v_lshlrev_b32 v38, 7, v38\n"
    "v_mfma_f32_16x16x32_bf16 v[68:71], v[100:103], v[188:191], v[68:71]\n"
    "v_bfe_u32 v39, v0, 4, 1\n"
    "v_lshl_add_u32 v38, v39, 5, v38\n"
    "v_and_b32 v39, 3, v0\n"
    "v_mfma_f32_16x16x32_bf16 v[72:75], v[104:107], v[24:27], v[72:75]\n"
    "v_lshl_add_u32 v38, v39, 3, v38\n"
    "s_lshl_b32 s69, s36, 11\n"
    "s_waitcnt lgkmcnt(0)\n"
    "v_mfma_f32_16x16x32_bf16 v[48:51], v[76:79], v[192:195], v[48:51]\n"
    "ds_read_b64_tr_b16 v[92:93], v9 offset:51200\n"
    "ds_read_b64_tr_b16 v[94:95], v9 offset:51456\n"
    "s_bfe_u32 s71, s0, 0x40010\n"
    "v_mfma_f32_16x16x32_bf16 v[52:55], v[76:79], v[196:199], v[52:55]\n"
    "ds_read_b64_tr_b16 v[96:97], v14 offset:61440\n"
    "ds_read_b64_tr_b16 v[98:99], v14 offset:61952\n"
    "s_mul_i32 s71, s71, 0x38000\n"
    "v_mfma_f32_16x16x32_bf16 v[56:59], v[80:83], v[192:195], v[56:59]\n"
    "ds_read_b64_tr_b16 v[100:101], v15 offset:61440\n"
    "ds_read_b64_tr_b16 v[102:103], v15 offset:61952\n"
    "s_mov_b32 m0, s45\n"
    "v_mfma_f32_16x16x32_bf16 v[60:63], v[80:83], v[196:199], v[60:63]\n"
    "ds_read_b64_tr_b16 v[104:105], v18 offset:51200\n"
    "ds_read_b64_tr_b16 v[106:107], v18 offset:51456\n"
    "s_add_u32 s71, s71, s3\n"
    "v_mfma_f32_16x16x32_bf16 v[64:67], v[84:87], v[192:195], v[64:67]\n"
    "buffer_load_dwordx4 v10, s[12:15], s71 offen lds\n"
    "v_add_u32 v38, s69, v38\n"
    "v_lshrrev_b32 v37, 1, v37\n"
    "v_mfma_f32_16x16x32_bf16 v[68:71], v[84:87], v[196:199], v[68:71]\n"
    "v_lshl_add_u32 v6, v37, 6, v38\n"
    "v_xor_b32 v7, 64, v6\n"
    "v_mfma_f32_16x16x32_bf16 v[72:75], v[88:91], v[24:27], v[72:75]\n"
    "s_waitcnt lgkmcnt(0)\n"
    "v_mfma_f32_16x16x32_bf16 v[48:51], v[92:95], v[200:203], v[48:51]\n"
    "ds_read_b64_tr_b16 v[76:77], v9 offset:53248\n"
    "ds_read_b64_tr_b16 v[78:79], v9 offset:53504\n"
    "v_mfma_f32_16x16x32_bf16 v[52:55], v[92:95], v[204:207], v[52:55]\n"
    "v_add_u32 v14, 0x10000, v14\n"
    "ds_read_b64_tr_b16 v[80:81], v14\n"
    "v_mfma_f32_16x16x32_bf16 v[56:59], v[96:99], v[200:203], v[56:59]\n"
    "ds_read_b64_tr_b16 v[82:83], v14 offset:512\n"
    "v_add_u32 v15, 0x10000, v15\n"
    "v_mfma_f32_16x16x32_bf16 v[60:63], v[96:99], v[204:207], v[60:63]\n"
    "ds_read_b64_tr_b16 v[84:85], v15\n"
    "ds_read_b64_tr_b16 v[86:87], v15 offset:512\n"
    "v_mfma_f32_16x16x32_bf16 v[64:67], v[100:103], v[200:203], v[64:67]\n"
    "ds_read_b64_tr_b16 v[88:89], v18 offset:53248\n"
    "ds_read_b64_tr_b16 v[90:91], v18 offset:53504\n"
    "v_mfma_f32_16x16x32_bf16 v[68:71], v[100:103], v[204:207], v[68:71]\n"
    "s_bfe_u32 s71, s0, 0x40014\n"
    "s_mul_i32 s71, s71, 0x38000\n"
    "s_add_u32 m0, s45, 0x2000\n"
    "v_mfma_f32_16x16x32_bf16 v[72:75], v[104:107], v[24:27], v[72:75]\n"
    "s_add_u32 s71, s71, s3\n"
    "buffer_load_dwordx4 v10, s[12:15], s71 offen lds\n"
    "s_waitcnt lgkmcnt(0)\n"
    "v_mfma_f32_16x16x32_bf16 v[48:51], v[76:79], v[208:211], v[48:51]\n"
    "ds_read_b64_tr_b16 v[92:93], v9 offset:55296\n"
    "ds_read_b64_tr_b16 v[94:95], v9 offset:55552\n"
    "v_mfma_f32_16x16x32_bf16 v[52:55], v[76:79], v[212:215], v[52:55]\n"
    "ds_read_b64_tr_b16 v[96:97], v14 offset:4096\n"
    "ds_read_b64_tr_b16 v[98:99], v14 offset:4608\n"
    "v_mfma_f32_16x16x32_bf16 v[56:59], v[80:83], v[208:211], v[56:59]\n"
    "ds_read_b64_tr_b16 v[100:101], v15 offset:4096\n"
    "ds_read_b64_tr_b16 v[102:103], v15 offset:4608\n"
    "v_mfma_f32_16x16x32_bf16 v[60:63], v[80:83], v[212:215], v[60:63]\n"
    "ds_read_b64_tr_b16 v[104:105], v18 offset:55296\n"
    "ds_read_b64_tr_b16 v[106:107], v18 offset:55552\n"
    "v_mfma_f32_16x16x32_bf16 v[64:67], v[84:87], v[208:211], v[64:67]\n"
    "s_bfe_u32 s71, s0, 0x40018\n"
    "s_mul_i32 s71, s71, 0x38000\n"
    "v_mfma_f32_16x16x32_bf16 v[68:71], v[84:87], v[212:215], v[68:71]\n"
    "s_add_u32 m0, s45, 0x4000\n"
    "s_add_u32 s71, s71, s3\n"
    "v_mfma_f32_16x16x32_bf16 v[72:75], v[88:91], v[24:27], v[72:75]\n"
    "buffer_load_dwordx4 v10, s[12:15], s71 offen lds\n"
    "s_waitcnt vmcnt(7) lgkmcnt(0)\n"
    "s_barrier\n"
    "v_mfma_f32_16x16x32_bf16 v[48:51], v[92:95], v[216:219], v[48:51]\n"
    "v_add_u32 v9, 0x10000, v9\n"
    "ds_read_b64_tr_b16 v[76:77], v9 offset:8192\n"
    "v_mfma_f32_16x16x32_bf16 v[52:55], v[92:95], v[220:223], v[52:55]\n"
    "ds_read_b64_tr_b16 v[78:79], v9 offset:8448\n"
    "ds_read_b64_tr_b16 v[80:81], v14 offset:16384\n"
    "v_mfma_f32_16x16x32_bf16 v[56:59], v[96:99], v[216:219], v[56:59]\n"
    "ds_read_b64_tr_b16 v[82:83], v14 offset:16896\n"
    "ds_read_b64_tr_b16 v[84:85], v15 offset:16384\n"
    "v_mfma_f32_16x16x32_bf16 v[60:63], v[96:99], v[220:223], v[60:63]\n"
    "ds_read_b64_tr_b16 v[86:87], v15 offset:16896\n"
    "v_add_u32 v18, 0x10000, v18\n"
    "v_mfma_f32_16x16x32_bf16 v[64:67], v[100:103], v[216:219], v[64:67]\n"
    "ds_read_b64_tr_b16 v[88:89], v18 offset:8192\n"
    "ds_read_b64_tr_b16 v[90:91], v18 offset:8448\n"
    "s_mov_b32 s71, 0x8000\n"
    "buffer_load_dword v124, v125, s[28:31], s71 offen sc1\n"
    "v_mfma_f32_16x16x32_bf16 v[68:71], v[100:103], v[220:223], v[68:71]\n"
    "s_bfe_u32 s71, s0, 0x4001c\n"
    "s_mul_i32 s71, s71, 0x38000\n"
    "s_add_u32 m0, s45, 0x6000\n"
    "v_mfma_f32_16x16x32_bf16 v[72:75], v[104:107], v[24:27], v[72:75]\n"
    "s_add_u32 s71, s71, s3\n"
    "buffer_load_dwordx4 v10, s[12:15], s71 offen lds\n"
    "s_waitcnt lgkmcnt(0)\n"
    "v_mfma_f32_16x16x32_bf16 v[48:51], v[76:79], v[224:227], v[48:51]\n"
    "ds_read_b64_tr_b16 v[92:93], v9 offset:10240\n"
    "ds_read_b64_tr_b16 v[94:95], v9 offset:10496\n"
    "v_mfma_f32_16x16x32_bf16 v[52:55], v[76:79], v[228:231], v[52:55]\n"
    "ds_read_b64_tr_b16 v[96:97], v14 offset:20480\n"
    "ds_read_b64_tr_b16 v[98:99], v14 offset:20992\n"
    "v_mfma_f32_16x16x32_bf16 v[56:59], v[80:83], v[224:227], v[56:59]\n"
    "ds_read_b64_tr_b16 v[100:101], v15 offset:20480\n"
    "ds_read_b64_tr_b16 v[102:103], v15 offset:20992\n"
    "v_mfma_f32_16x16x32_bf16 v[60:63], v[80:83], v[228:231], v[60:63]\n"
    "ds_read_b64_tr_b16 v[104:105], v18 offset:10240\n"
    "ds_read_b64_tr_b16 v[106:107], v18 offset:10496\n"
    "v_mfma_f32_16x16x32_bf16 v[64:67], v[84:87], v[224:227], v[64:67]\n"
    "s_add_u32 m0, s61, 0xc000\n"
    "s_mov_b32 s71, s59\n"
    "v_mfma_f32_16x16x32_bf16 v[68:71], v[84:87], v[228:231], v[68:71]\n"
    "buffer_load_dwordx4 v5, s[16:19], s71 offen lds\n"
    "v_mfma_f32_16x16x32_bf16 v[72:75], v[88:91], v[24:27], v[72:75]\n"
    "s_waitcnt lgkmcnt(0)\n"
    "v_mfma_f32_16x16x32_bf16 v[48:51], v[92:95], v[232:235], v[48:51]\n"
    "ds_read_b64_tr_b16 v[76:77], v9 offset:12288\n"
    "ds_read_b64_tr_b16 v[78:79], v9 offset:12544\n"
    "v_mfma_f32_16x16x32_bf16 v[52:55], v[92:95], v[236:239], v[52:55]\n"
    "ds_read_b64_tr_b16 v[80:81], v14 offset:24576\n"
    "ds_read_b64_tr_b16 v[82:83], v14 offset:25088\n"
    "v_mfma_f32_16x16x32_bf16 v[56:59], v[96:99], v[232:235], v[56:59]\n"
    "ds_read_b64_tr_b16 v[84:85], v15 offset:24576\n"
    "ds_read_b64_tr_b16 v[86:87], v15 offset:25088\n"
    "v_mfma_f32_16x16x32_bf16 v[60:63], v[96:99], v[236:239], v[60:63]\n"
    "ds_read_b64_tr_b16 v[88:89], v18 offset:12288\n"
    "ds_read_b64_tr_b16 v[90:91], v18 offset:12544\n"
    "v_mfma_f32_16x16x32_bf16 v[64:67], v[100:103], v[232:235], v[64:67]\n"
    "s_waitcnt vmcnt(2)\n"
    "v_cmp_ne_u32 vcc, v22, v124\n"
    "s_cbranch_vccnz .Lk_spin_3\n"
    ".Lk_ok_4:\n"
    "buffer_load_dwordx4 v[108:111], v126, s[28:31], s53 offen sc1\n"
    "v_mfma_f32_16x16x32_bf16 v[68:71], v[100:103], v[236:239], v[68:71]\n"
    "buffer_load_dwordx4 v[112:115], v126, s[28:31], s53 offen offset:1024 sc1\n"
    "s_add_u32 m0, s61, 0xe000\n"
    "v_mfma_f32_16x16x32_bf16 v[72:75], v[104:107], v[24:27], v[72:75]\n"
    "s_add_u32 s71, s59, 0x4000\n"
    "buffer_load_dwordx4 v5, s[16:19], s71 offen lds\n"
    "s_waitcnt lgkmcnt(0)\n"
    "v_mfma_f32_16x16x32_bf16 v[48:51], v[76:79], v[240:243], v[48:51]\n"
    "ds_read_b64_tr_b16 v[92:93], v9 offset:14336\n"
    "ds_read_b64_tr_b16 v[94:95], v9 offset:14592\n"
    "v_mfma_f32_16x16x32_bf16 v[52:55], v[76:79], v[244:247], v[52:55]\n"
    "ds_read_b64_tr_b16 v[96:97], v14 offset:28672\n"
    "ds_read_b64_tr_b16 v[98:99], v14 offset:29184\n"
    "v_mfma_f32_16x16x32_bf16 v[56:59], v[80:83], v[240:243], v[56:59]\n"
    "ds_read_b64_tr_b16 v[100:101], v15 offset:28672\n"
    "ds_read_b64_tr_b16 v[102:103], v15 offset:29184\n"
    "v_mfma_f32_16x16x32_bf16 v[60:63], v[80:83], v[244:247], v[60:63]\n"
    "ds_read_b64_tr_b16 v[104:105], v18 offset:14336\n"
    "ds_read_b64_tr_b16 v[106:107], v18 offset:14592\n"
    "v_mfma_f32_16x16x32_bf16 v[64:67], v[84:87], v[240:243], v[64:67]\n"
    "buffer_load_dwordx4 v[116:119], v127, s[28:31], s54 offen sc1\n"
    "buffer_load_dwordx4 v[120:123], v127, s[28:31], s54 offen offset:1024 sc1\n"
    "v_mfma_f32_16x16x32_bf16 v[68:71], v[84:87], v[244:247], v[68:71]\n"
    "s_add_u32 m0, s61, 0x10000\n"
    "s_add_u32 s71, s59, 0x8000\n"
    "v_mfma_f32_16x16x32_bf16 v[72:75], v[88:91], v[24:27], v[72:75]\n"
    "buffer_load_dwordx4 v5, s[16:19], s71 offen lds\n"
    "s_waitcnt lgkmcnt(0)\n"
    "v_mfma_f32_16x16x32_bf16 v[72:75], v[104:107], v[24:27], v[72:75]\n"
    "buffer_load_dwordx4 v[124:127], v1, s[28:31], s42 offen sc1\n"
    "v_mfma_f32_16x16x32_bf16 v[48:51], v[92:95], v[248:251], v[48:51]\n"
    "v_mfma_f32_16x16x32_bf16 v[52:55], v[92:95], v[252:255], v[52:55]\n"
    "v_mfma_f32_16x16x32_bf16 v[56:59], v[96:99], v[248:251], v[56:59]\n"
    "v_mfma_f32_16x16x32_bf16 v[60:63], v[96:99], v[252:255], v[60:63]\n"
    "v_mfma_f32_16x16x32_bf16 v[64:67], v[100:103], v[248:251], v[64:67]\n"
    "v_mfma_f32_16x16x32_bf16 v[68:71], v[100:103], v[252:255], v[68:71]\n"
    "s_mov_b32 exec_lo, 0x10001\n"
    "s_mov_b32 exec_hi, 0x10001\n"
    "ds_write_b128 v11, v[72:75] offset:512\n"
    "s_mov_b64 exec, -1\n"
    "s_waitcnt lgkmcnt(0)\n"
    "s_barrier\n"
    "ds_read_b128 v[36:39], v12 offset:768\n"
    "v_add_u32 v16, 0x10000, v16\n"
    "ds_read_b64_tr_b16 v[76:77], v16 offset:32768\n"
    "ds_read_b64_tr_b16 v[78:79], v16 offset:33280\n"
    "v_add_u32 v17, 0x10000, v17\n"
    "ds_read_b64_tr_b16 v[80:81], v17 offset:32768\n"
    "ds_read_b64_tr_b16 v[82:83], v17 offset:33280\n"
    "ds_read_b64_tr_b16 v[84:85], v16 offset:36864\n"
    "ds_read_b64_tr_b16 v[86:87], v16 offset:37376\n"
    "ds_read_b64_tr_b16 v[88:89], v17 offset:36864\n"
    "ds_read_b64_tr_b16 v[90:91], v17 offset:37376\n"
    "s_add_u32 m0, s61, 0x12000\n"
    "s_add_u32 s71, s59, 0xc000\n"
    "buffer_load_dwordx4 v5, s[16:19], s71 offen lds\n"
    "s_waitcnt lgkmcnt(8)\n"
    "v_pk_mul_f32 v[56:57], v[56:57], v[42:43] op_sel_hi:[1,0]\n"
    "v_pk_mul_f32 v[58:59], v[58:59], v[42:43] op_sel_hi:[1,0]\n"
    "v_pk_fma_f32 v[56:57], v[36:37], v[40:41], v[56:57] op_sel_hi:[1,0,1]\n"
    "v_pk_fma_f32 v[58:59], v[38:39], v[40:41], v[58:59] op_sel_hi:[1,0,1]\n"
    "v_pk_mul_f32 v[60:61], v[60:61], v[46:47] op_sel_hi:[1,0]\n"
    "v_pk_mul_f32 v[62:63], v[62:63], v[46:47] op_sel_hi:[1,0]\n"
    "v_pk_fma_f32 v[60:61], v[36:37], v[44:45], v[60:61] op_sel_hi:[1,0,1]\n"
    "v_pk_fma_f32 v[62:63], v[38:39], v[44:45], v[62:63] op_sel_hi:[1,0,1]\n"
    "ds_read_b128 v[36:39], v12 offset:896\n"
    "v_cvt_pk_bf16_f32 v56, v56, v57\n"
    "v_cvt_pk_bf16_f32 v57, v58, v59\n"
    "v_cvt_pk_bf16_f32 v58, v60, v61\n"
    "v_cvt_pk_bf16_f32 v59, v62, v63\n"
    "v_pk_max_i16 v56, v56, 0\n"
    "v_pk_max_i16 v57, v57, 0\n"
    "v_pk_max_i16 v58, v58, 0\n"
    "v_pk_max_i16 v59, v59, 0\n"
    "s_nop 0\n"
    "v_permlane16_swap_b32 v56, v58\n"
    "v_permlane16_swap_b32 v57, v59\n"
    "buffer_store_dwordx4 v[56:59], v1, s[28:31], s50 offen sc1\n"
    "s_waitcnt lgkmcnt(0)\n"
    "v_pk_mul_f32 v[64:65], v[64:65], v[42:43] op_sel_hi:[1,0]\n"
    "v_pk_mul_f32 v[66:67], v[66:67], v[42:43] op_sel_hi:[1,0]\n"
    "v_pk_fma_f32 v[64:65], v[36:37], v[40:41], v[64:65] op_sel_hi:[1,0,1]\n"
    "v_pk_fma_f32 v[66:67], v[38:39], v[40:41], v[66:67] op_sel_hi:[1,0,1]\n"
    "v_pk_mul_f32 v[68:69], v[68:69], v[46:47] op_sel_hi:[1,0]\n"
    "v_pk_mul_f32 v[70:71], v[70:71], v[46:47] op_sel_hi:[1,0]\n"
    "v_pk_fma_f32 v[68:69], v[36:37], v[44:45], v[68:69] op_sel_hi:[1,0,1]\n"
    "v_pk_fma_f32 v[70:71], v[38:39], v[44:45], v[70:71] op_sel_hi:[1,0,1]\n"
    "ds_read_b128 v[36:39], v12 offset:512\n"
    "v_cvt_pk_bf16_f32 v64, v64, v65\n"
    "v_cvt_pk_bf16_f32 v65, v66, v67\n"
    "v_cvt_pk_bf16_f32 v66, v68, v69\n"
    "v_cvt_pk_bf16_f32 v67, v70, v71\n"
    "v_pk_max_i16 v64, v64, 0\n"
    "v_pk_max_i16 v65, v65, 0\n"
    "v_pk_max_i16 v66, v66, 0\n"
    "v_pk_max_i16 v67, v67, 0\n"
    "s_nop 0\n"
    "v_permlane16_swap_b32 v64, v66\n"
    "v_permlane16_swap_b32 v65, v67\n"
    "buffer_store_dwordx4 v[64:67], v1, s[28:31], s50 offen offset:2048 sc1\n"
    "v_mul_f32 v60, 0x3eb8aa3b, v42\n"
    "v_mul_f32 v62, 0x3eb8aa3b, v40\n"
    "v_mul_f32 v68, 0x3eb8aa3b, v46\n"
    "v_mul_f32 v70, 0x3eb8aa3b, v44\n"
    "s_waitcnt lgkmcnt(0)\n"
    "v_pk_mul_f32 v[48:49], v[48:49], v[60:61] op_sel_hi:[1,0]\n"
    "v_pk_mul_f32 v[50:51], v[50:51], v[60:61] op_sel_hi:[1,0]\n"
    "v_pk_fma_f32 v[48:49], v[36:37], v[62:63], v[48:49] op_sel_hi:[1,0,1]\n"
    "v_pk_fma_f32 v[50:51], v[38:39], v[62:63], v[50:51] op_sel_hi:[1,0,1]\n"
    "v_pk_mul_f32 v[52:53], v[52:53], v[68:69] op_sel_hi:[1,0]\n"
    "v_pk_mul_f32 v[54:55], v[54:55], v[68:69] op_sel_hi:[1,0]\n"
    "v_pk_fma_f32 v[52:53], v[36:37], v[70:71], v[52:53] op_sel_hi:[1,0,1]\n"
    "v_pk_fma_f32 v[54:55], v[38:39], v[70:71], v[54:55] op_sel_hi:[1,0,1]\n"
    "v_cvt_pk_bf16_f32 v28, v48, v49\n"
    "v_cvt_pk_bf16_f32 v29, v50, v51\n"
    "v_cvt_pk_bf16_f32 v30, v52, v53\n"
    "v_cvt_pk_bf16_f32 v31, v54, v55\n"
    "s_nop 0\n"
    "v_permlane16_swap_b32 v28, v30\n"
    "v_permlane16_swap_b32 v29, v31\n"
    "s_waitcnt vmcnt(3)\n"
    "ds_write_b128 v19, v[108:111]\n"
    "ds_write_b128 v20, v[112:115]\n"
    "ds_write_b128 v19, v[116:119] offset:16384\n"
    "ds_write_b128 v20, v[120:123] offset:16384\n"
    "ds_write_b128 v21, v[124:127] offset:32768\n"
    "v_cndmask_b32 v32, v32, v124, s[72:73]\n"
    "v_cndmask_b32 v33, v33, v125, s[72:73]\n"
    "v_cndmask_b32 v34, v34, v126, s[72:73]\n"
    "v_cndmask_b32 v35, v35, v127, s[72:73]\n"
    "v_mfma_f32_16x16x32_bf16 v[108:111], v[76:79], v[128:131], 0\n"
    "ds_read_b64_tr_b16 v[92:93], v16 offset:40960\n"
    "ds_read_b64_tr_b16 v[94:95], v16 offset:41472\n"
    "v_mfma_f32_16x16x32_bf16 v[112:115], v[76:79], v[132:135], 0\n"
    "ds_read_b64_tr_b16 v[96:97], v17 offset:40960\n"
    "ds_read_b64_tr_b16 v[98:99], v17 offset:41472\n"
    "v_mfma_f32_16x16x32_bf16 v[116:119], v[80:83], v[128:131], 0\n"
    "ds_read_b64_tr_b16 v[100:101], v16 offset:45056\n"
    "ds_read_b64_tr_b16 v[102:103], v16 offset:45568\n"
    "v_mfma_f32_16x16x32_bf16 v[120:123], v[80:83], v[132:135], 0\n"
    "ds_read_b64_tr_b16 v[104:105], v17 offset:45056\n"
    "ds_read_b64_tr_b16 v[106:107], v17 offset:45568\n"
    "v_mfma_f32_16x16x32_bf16 v[108:111], v[84:87], v[136:139], v[108:111]\n"
    "v_mfma_f32_16x16x32_bf16 v[112:115], v[84:87], v[140:143], v[112:115]\n"
    "v_mfma_f32_16x16x32_bf16 v[116:119], v[88:91], v[136:139], v[116:119]\n"
    "v_mfma_f32_16x16x32_bf16 v[120:123], v[88:91], v[140:143], v[120:123]\n"
    "s_waitcnt lgkmcnt(0)\n"
    "s_barrier\n"
    "v_mfma_f32_16x16x32_bf16 v[108:111], v[92:95], v[144:147], v[108:111]\n"
    "v_add_u32 v16, 0xffff0000, v16\n"
    "ds_read_b64_tr_b16 v[76:77], v16 offset:32768\n"
    "v_mfma_f32_16x16x32_bf16 v[112:115], v[92:95], v[148:151], v[112:115]\n"
    "ds_read_b64_tr_b16 v[78:79], v16 offset:33280\n"
    "v_add_u32 v17, 0xffff0000, v17\n"
    "v_mfma_f32_16x16x32_bf16 v[116:119], v[96:99], v[144:147], v[116:119]\n"
    "ds_read_b64_tr_b16 v[80:81], v17 offset:32768\n"
    "ds_read_b64_tr_b16 v[82:83], v17 offset:33280\n"
    "v_mfma_f32_16x16x32_bf16 v[120:123], v[96:99], v[148:151], v[120:123]\n"
    "ds_read_b64_tr_b16 v[84:85], v16 offset:36864\n"
    "ds_read_b64_tr_b16 v[86:87], v16 offset:37376\n"
    "v_mfma_f32_16x16x32_bf16 v[108:111], v[100:103], v[152:155], v[108:111]\n"
    "ds_read_b64_tr_b16 v[88:89], v17 offset:36864\n"
    "ds_read_b64_tr_b16 v[90:91], v17 offset:37376\n"
    "v_mfma_f32_16x16x32_bf16 v[112:115], v[100:103], v[156:159], v[112:115]\n"
    "v_mfma_f32_16x16x32_bf16 v[116:119], v[104:107], v[152:155], v[116:119]\n"
    "v_mfma_f32_16x16x32_bf16 v[120:123], v[104:107], v[156:159], v[120:123]\n"
    "s_waitcnt lgkmcnt(0)\n"
    "v_mfma_f32_16x16x32_bf16 v[108:111], v[76:79], v[160:163], v[108:111]\n"
    "ds_read_b64_tr_b16 v[92:93], v16 offset:40960\n"
    "ds_read_b64_tr_b16 v[94:95], v16 offset:41472\n"
    "v_mfma_f32_16x16x32_bf16 v[112:115], v[76:79], v[164:167], v[112:115]\n"
    "ds_read_b64_tr_b16 v[96:97], v17 offset:40960\n"
    "ds_read_b64_tr_b16 v[98:99], v17 offset:41472\n"
    "v_mfma_f32_16x16x32_bf16 v[116:119], v[80:83], v[160:163], v[116:119]\n"
    "ds_read_b64_tr_b16 v[100:101], v16 offset:45056\n"
    "ds_read_b64_tr_b16 v[102:103], v16 offset:45568\n"
    "v_mfma_f32_16x16x32_bf16 v[120:123], v[80:83], v[164:167], v[120:123]\n"
    "ds_read_b64_tr_b16 v[104:105], v17 offset:45056\n"
    "ds_read_b64_tr_b16 v[106:107], v17 offset:45568\n"
    "v_mfma_f32_16x16x32_bf16 v[108:111], v[84:87], v[168:171], v[108:111]\n"
    "s_add_u32 m0, s61, 0x14000\n"
    "s_add_u32 s71, s59, 0x20000\n"
    "v_mfma_f32_16x16x32_bf16 v[112:115], v[84:87], v[172:175], v[112:115]\n"
    "buffer_load_dwordx4 v5, s[16:19], s71 offen lds\n"
    "s_add_u32 m0, s61, 0x16000\n"
    "v_mfma_f32_16x16x32_bf16 v[116:119], v[88:91], v[168:171], v[116:119]\n"
    "s_add_u32 s71, s59, 0x24000\n"
    "buffer_load_dwordx4 v5, s[16:19], s71 offen lds\n"
    "v_mfma_f32_16x16x32_bf16 v[120:123], v[88:91], v[172:175], v[120:123]\n"
    "s_waitcnt lgkmcnt(0)\n"
    "s_barrier\n"
    "v_mfma_f32_16x16x32_bf16 v[108:111], v[92:95], v[176:179], v[108:111]\n"
    "ds_read_b64_tr_b16 v[76:77], v16\n"
    "ds_read_b64_tr_b16 v[78:79], v16 offset:512\n"
    "s_add_u32 m0, s61, 0x18000\n"
    "v_mfma_f32_16x16x32_bf16 v[112:115], v[92:95], v[180:183], v[112:115]\n"
    "ds_read_b64_tr_b16 v[80:81], v17\n"
    "ds_read_b64_tr_b16 v[82:83], v17 offset:512\n"
    "s_add_u32 s71, s59, 0x28000\n"
    "v_mfma_f32_16x16x32_bf16 v[116:119], v[96:99], v[176:179], v[116:119]\n"
    "ds_read_b64_tr_b16 v[84:85], v16 offset:4096\n"
    "ds_read_b64_tr_b16 v[86:87], v16 offset:4608\n"
    "buffer_load_dwordx4 v5, s[16:19], s71 offen lds\n"
    "v_mfma_f32_16x16x32_bf16 v[120:123], v[96:99], v[180:183], v[120:123]\n"
    "ds_read_b64_tr_b16 v[88:89], v17 offset:4096\n"
    "ds_read_b64_tr_b16 v[90:91], v17 offset:4608\n"
    "v_and_b32 v169, 31, v0\n"
    "v_mfma_f32_16x16x32_bf16 v[108:111], v[100:103], v[184:187], v[108:111]\n"
    "v_lshrrev_b32 v170, 5, v0\n"
    "v_lshlrev_b32 v171, 2, v170\n"
    "v_sub_u32 v169, v169, v171\n"
    "v_mfma_f32_16x16x32_bf16 v[112:115], v[100:103], v[188:191], v[112:115]\n"
    "v_cvt_f32_i32 v169, v169\n"
    "v_mul_f32 v169, 0x4e800000, v169\n"
    "v_add_f32 v128, 0, v169\n"
    "v_mfma_f32_16x16x32_bf16 v[116:119], v[104:107], v[184:187], v[116:119]\n"
    "v_med3_f32 v128, v128, s75, 0\n"
    "v_sub_f32 v144, s75, v128\n"
    "v_add_f32 v129, 0xce800000, v169\n"
    "v_mfma_f32_16x16x32_bf16 v[120:123], v[104:107], v[188:191], v[120:123]\n"
    "v_med3_f32 v129, v129, s75, 0\n"
    "v_sub_f32 v145, s75, v129\n"
    "s_waitcnt lgkmcnt(0)\n"
    "v_mfma_f32_16x16x32_bf16 v[108:111], v[76:79], v[192:195], v[108:111]\n"
    "ds_read_b64_tr_b16 v[92:93], v16 offset:8192\n"
    "ds_read_b64_tr_b16 v[94:95], v16 offset:8704\n"
    "s_waitcnt vmcnt(3)\n"
    "s_add_u32 s71, s49, 0x10000\n"
    "s_mov_b64 exec, 1\n"
    "buffer_store_dword v22, off, s[28:31], s71 sc1\n"
    "s_mov_b64 exec, -1\n"
    "v_mfma_f32_16x16x32_bf16 v[112:115], v[76:79], v[196:199], v[112:115]\n"
    "ds_read_b64_tr_b16 v[96:97], v17 offset:8192\n"
    "ds_read_b64_tr_b16 v[98:99], v17 offset:8704\n"
    "s_add_u32 m0, s61, 0x1a000\n"
    "v_mfma_f32_16x16x32_bf16 v[116:119], v[80:83], v[192:195], v[116:119]\n"
    "ds_read_b64_tr_b16 v[100:101], v16 offset:12288\n"
    "ds_read_b64_tr_b16 v[102:103], v16 offset:12800\n"
    "s_add_u32 s71, s59, 0x2c000\n"
    "v_mfma_f32_16x16x32_bf16 v[120:123], v[80:83], v[196:199], v[120:123]\n"
    "ds_read_b64_tr_b16 v[104:105], v17 offset:12288\n"
    "ds_read_b64_tr_b16 v[106:107], v17 offset:12800\n"
    "buffer_load_dwordx4 v5, s[16:19], s71 offen lds\n"
    "v_mfma_f32_16x16x32_bf16 v[108:111], v[84:87], v[200:203], v[108:111]\n"
    "s_add_u32 m0, s61, 0x8000\n"
    "s_add_u32 s71, s59, 0x40000\n"
    "buffer_load_dwordx4 v5, s[16:19], s71 offen lds\n"
    "v_mfma_f32_16x16x32_bf16 v[112:115], v[84:87], v[204:207], v[112:115]\n"
    "v_add_f32 v130, 0xcf000000, v169\n"
    "v_med3_f32 v130, v130, s75, 0\n"
    "v_sub_f32 v146, s75, v130\n"
    "v_mfma_f32_16x16x32_bf16 v[116:119], v[88:91], v[200:203], v[116:119]\n"
    "v_add_f32 v131, 0xcf400000, v169\n"
    "v_med3_f32 v131, v131, s75, 0\n"
    "v_sub_f32 v147, s75, v131\n"
    "v_mfma_f32_16x16x32_bf16 v[120:123], v[88:91], v[204:207], v[120:123]\n"
    "v_add_f32 v132, 0xd0000000, v169\n"
    "v_med3_f32 v132, v132, s75, 0\n"
    "s_waitcnt lgkmcnt(0)\n"
    "s_barrier\n"
    "v_mfma_f32_16x16x32_bf16 v[108:111], v[92:95], v[208:211], v[108:111]\n"
    "ds_read_b64_tr_b16 v[76:77], v16 offset:16384\n"
    "ds_read_b64_tr_b16 v[78:79], v16 offset:16896\n"
    "s_add_u32 m0, s61, 0xa000\n"
    "v_mfma_f32_16x16x32_bf16 v[112:115], v[92:95], v[212:215], v[112:115]\n"
    "ds_read_b64_tr_b16 v[80:81], v17 offset:16384\n"
    "ds_read_b64_tr_b16 v[82:83], v17 offset:16896\n"
    "s_add_u32 s71, s59, 0x44000\n"
    "v_mfma_f32_16x16x32_bf16 v[116:119], v[96:99], v[208:211], v[116:119]\n"
    "ds_read_b64_tr_b16 v[84:85], v16 offset:20480\n"
    "ds_read_b64_tr_b16 v[86:87], v16 offset:20992\n"
    "buffer_load_dwordx4 v5, s[16:19], s71 offen lds\n"
    "v_mfma_f32_16x16x32_bf16 v[120:123], v[96:99], v[212:215], v[120:123]\n"
    "ds_read_b64_tr_b16 v[88:89], v17 offset:20480\n"
    "ds_read_b64_tr_b16 v[90:91], v17 offset:20992\n"
    "s_mov_b32 m0, s61\n"
    "v_mfma_f32_16x16x32_bf16 v[108:111], v[100:103], v[216:219], v[108:111]\n"
    "s_add_u32 s71, s59, 0x48000\n"
    "buffer_load_dwordx4 v5, s[16:19], s71 offen lds\n"
    "v_sub_f32 v148, s75, v132\n"
    "v_mfma_f32_16x16x32_bf16 v[112:115], v[100:103], v[220:223], v[112:115]\n"
    "v_add_f32 v133, 0xd0100000, v169\n"
    "v_med3_f32 v133, v133, s75, 0\n"
    "v_sub_f32 v149, s75, v133\n"
    "v_mfma_f32_16x16x32_bf16 v[116:119], v[104:107], v[216:219], v[116:119]\n"
    "v_add_f32 v134, 0xd0200000, v169\n"
    "v_med3_f32 v134, v134, s75, 0\n"
    "v_sub_f32 v150, s75, v134\n"
    "v_mfma_f32_16x16x32_bf16 v[120:123], v[104:107], v[220:223], v[120:123]\n"
    "v_add_f32 v135, 0xd0300000, v169\n"
    "v_med3_f32 v135, v135, s75, 0\n"
    "s_waitcnt lgkmcnt(0)\n"
    "v_mfma_f32_16x16x32_bf16 v[108:111], v[76:79], v[224:227], v[108:111]\n"
    "ds_read_b64_tr_b16 v[92:93], v16 offset:24576\n"
    "ds_read_b64_tr_b16 v[94:95], v16 offset:25088\n"
    "s_add_u32 m0, s61, 0x2000\n"
    "v_mfma_f32_16x16x32_bf16 v[112:115], v[76:79], v[228:231], v[112:115]\n"
    "ds_read_b64_tr_b16 v[96:97], v17 offset:24576\n"
    "ds_read_b64_tr_b16 v[98:99], v17 offset:25088\n"
    "s_add_u32 s71, s59, 0x4c000\n"
    "v_mfma_f32_16x16x32_bf16 v[116:119], v[80:83], v[224:227], v[116:119]\n"
    "ds_read_b64_tr_b16 v[100:101], v16 offset:28672\n"
    "ds_read_b64_tr_b16 v[102:103], v16 offset:29184\n"
    "buffer_load_dwordx4 v5, s[16:19], s71 offen lds\n"
    "v_mfma_f32_16x16x32_bf16 v[120:123], v[80:83], v[228:231], v[120:123]\n"
    "ds_read_b64_tr_b16 v[104:105], v17 offset:28672\n"
    "ds_read_b64_tr_b16 v[106:107], v17 offset:29184\n"
    "v_sub_f32 v151, s75, v135\n"
    "v_mfma_f32_16x16x32_bf16 v[108:111], v[84:87], v[232:235], v[108:111]\n"
    "v_add_f32 v136, 0xd0800000, v169\n"
    "v_med3_f32 v136, v136, s75, 0\n"
    "v_sub_f32 v152, s75, v136\n"
    "v_mfma_f32_16x16x32_bf16 v[112:115], v[84:87], v[236:239], v[112:115]\n"
    "v_add_f32 v137, 0xd0880000, v169\n"
    "v_med3_f32 v137, v137, s75, 0\n"
    "v_sub_f32 v153, s75, v137\n"
    "v_mfma_f32_16x16x32_bf16 v[116:119], v[88:91], v[232:235], v[116:119]\n"
    "v_add_f32 v138, 0xd0900000, v169\n"
    "v_med3_f32 v138, v138, s75, 0\n"
    "v_sub_f32 v154, s75, v138\n"
    "v_mfma_f32_16x16x32_bf16 v[120:123], v[88:91], v[236:239], v[120:123]\n"
    "v_add_f32 v139, 0xd0980000, v169\n"
    "v_med3_f32 v139, v139, s75, 0\n"
    "s_waitcnt lgkmcnt(0)\n"
    "v_mfma_f32_16x16x32_bf16 v[108:111], v[92:95], v[240:243], v[108:111]\n"
    "v_sub_f32 v155, s75, v139\n"
    "v_add_f32 v140, 0xd0c00000, v169\n"
    "v_med3_f32 v140, v140, s75, 0\n"
    "v_mfma_f32_16x16x32_bf16 v[112:115], v[92:95], v[244:247], v[112:115]\n"
    "v_sub_f32 v156, s75, v140\n"
    "v_add_f32 v141, 0xd0c80000, v169\n"
    "v_med3_f32 v141, v141, s75, 0\n"
    "v_mfma_f32_16x16x32_bf16 v[116:119], v[96:99], v[240:243], v[116:119]\n"
    "v_sub_f32 v157, s75, v141\n"
    "v_add_f32 v142, 0xd0d00000, v169\n"
    "v_med3_f32 v142, v142, s75, 0\n"
    "v_mfma_f32_16x16x32_bf16 v[120:123], v[96:99], v[244:247], v[120:123]\n"
    "v_sub_f32 v158, s75, v142\n"
    "v_add_f32 v143, 0xd0d80000, v169\n"
    "v_med3_f32 v143, v143, s75, 0\n"
    "v_mfma_f32_16x16x32_bf16 v[108:111], v[100:103], v[248:251], v[108:111]\n"
    "v_sub_f32 v159, s75, v143\n"
    "v_bfe_u32 v171, v0, 4, 1\n"
    "v_xor_b32 v169, v170, v171\n"
    "v_mfma_f32_16x16x32_bf16 v[112:115], v[100:103], v[252:255], v[112:115]\n"
    "v_lshlrev_b32 v169, 7, v169\n"
    "v_and_b32 v170, 15, v0\n"
    "v_lshl_add_u32 v169, v170, 3, v169\n"
    "v_mfma_f32_16x16x32_bf16 v[116:119], v[104:107], v[248:251], v[116:119]\n"
    "s_lshl_b32 s66, s36, 12\n"
    "s_lshl_b32 s69, s37, 11\n"
    "s_add_u32 s66, s66, s69\n"
    "v_mfma_f32_16x16x32_bf16 v[120:123], v[104:107], v[252:255], v[120:123]\n"
    "s_add_u32 s66, s66, 0x24400\n"
    "s_sub_u32 s69, 0x27000, s66\n"
    "v_add_u32 v169, s66, v169\n"
    "v_mad_i32_i24 v160, v171, s69, v169\n"
    "v_add_u32 v170, -1, v171\n"
    "v_and_b32 v170, 0xfffff000, v170\n"
    "s_cmp_eq_u32 s65, 0\n"
    "s_cselect_b32 s66, 0xffff5000, 0\n"
    "v_add_u32 v161, v160, v170\n"
    "v_mad_i32_i24 v161, v171, s66, v161\n"
    "s_cmp_eq_u32 s65, 1\n"
    "s_cselect_b32 s66, 0xffff5000, 0\n"
    "v_add_u32 v162, v161, v170\n"
    "v_mad_i32_i24 v162, v171, s66, v162\n"
    "s_cmp_eq_u32 s65, 2\n"
    "s_cselect_b32 s66, 0xffff5000, 0\n"
    "v_add_u32 v163, v162, v170\n"
    "v_mad_i32_i24 v163, v171, s66, v163\n"
    "s_cmp_eq_u32 s65, 3\n"
    "s_cselect_b32 s66, 0xffff5000, 0\n"
    "v_add_u32 v164, v163, v170\n"
    "v_mad_i32_i24 v164, v171, s66, v164\n"
    "s_cmp_eq_u32 s65, 4\n"
    "s_cselect_b32 s66, 0xffff5000, 0\n"
    "v_add_u32 v165, v164, v170\n"
    "v_mad_i32_i24 v165, v171, s66, v165\n"
    "s_cmp_eq_u32 s65, 5\n"
    "s_cselect_b32 s66, 0xffff5000, 0\n"
    "v_add_u32 v166, v165, v170\n"
    "v_mad_i32_i24 v166, v171, s66, v166\n"
    "s_cmp_eq_u32 s65, 6\n"
    "s_cselect_b32 s66, 0xffff5000, 0\n"
    "v_add_u32 v167, v166, v170\n"
    "v_mad_i32_i24 v167, v171, s66, v167\n"
    "s_cmp_eq_u32 s65, 7\n"
    "s_cselect_b32 s66, 0xffff5000, 0\n"
    "v_add_u32 v168, v167, v170\n"
    "v_mad_i32_i24 v168, v171, s66, v168\n"
    "s_nop 3\n"
    "s_waitcnt vmcnt(7) lgkmcnt(0)\n"
    "s_barrier\n"
    "ds_read_b128 v[36:39], v13 offset:256\n"
    "ds_read_b128 v[104:107], v13 offset:384\n"
    "s_add_u32 s71, s62, 0x10000\n"
    "buffer_load_dword v124, v2, s[28:31], s71 offen sc1\n"
    "s_waitcnt lgkmcnt(1)\n"
    "v_pk_mul_f32 v[108:109], v[108:109], v[42:43] op_sel_hi:[1,0]\n"
    "v_pk_mul_f32 v[110:111], v[110:111], v[42:43] op_sel_hi:[1,0]\n"
    "v_pk_fma_f32 v[108:109], v[36:37], v[40:41], v[108:109] op_sel_hi:[1,0,1]\n"
    "v_pk_fma_f32 v[110:111], v[38:39], v[40:41], v[110:111] op_sel_hi:[1,0,1]\n"
    "v_pk_mul_f32 v[112:113], v[112:113], v[46:47] op_sel_hi:[1,0]\n"
    "v_pk_mul_f32 v[114:115], v[114:115], v[46:47] op_sel_hi:[1,0]\n"
    "v_pk_fma_f32 v[112:113], v[36:37], v[44:45], v[112:113] op_sel_hi:[1,0,1]\n"
    "v_pk_fma_f32 v[114:115], v[38:39], v[44:45], v[114:115] op_sel_hi:[1,0,1]\n"
    "v_cvt_pk_bf16_f32 v96, v108, v109\n"
    "v_cvt_pk_bf16_f32 v97, v110, v111\n"
    "v_cvt_pk_bf16_f32 v98, v112, v113\n"
    "v_cvt_pk_bf16_f32 v99, v114, v115\n"
    "v_pk_max_i16 v96, v96, 0\n"
    "v_pk_max_i16 v97, v97, 0\n"
    "v_pk_max_i16 v98, v98, 0\n"
    "v_pk_max_i16 v99, v99, 0\n"
    "s_nop 0\n"
    "v_permlane16_swap_b32 v96, v98\n"
    "v_permlane16_swap_b32 v97, v99\n"
    "buffer_store_dwordx4 v[96:99], v1, s[28:31], s51 offen sc1\n"
    "s_waitcnt lgkmcnt(0)\n"
    "v_pk_mul_f32 v[116:117], v[116:117], v[42:43] op_sel_hi:[1,0]\n"
    "v_pk_mul_f32 v[118:119], v[118:119], v[42:43] op_sel_hi:[1,0]\n"
    "v_pk_fma_f32 v[116:117], v[104:105], v[40:41], v[116:117] op_sel_hi:[1,0,1]\n"
    "v_pk_fma_f32 v[118:119], v[106:107], v[40:41], v[118:119] op_sel_hi:[1,0,1]\n"
    "v_pk_mul_f32 v[120:121], v[120:121], v[46:47] op_sel_hi:[1,0]\n"
    "v_pk_mul_f32 v[122:123], v[122:123], v[46:47] op_sel_hi:[1,0]\n"
    "v_pk_fma_f32 v[120:121], v[104:105], v[44:45], v[120:121] op_sel_hi:[1,0,1]\n"
    "v_pk_fma_f32 v[122:123], v[106:107], v[44:45], v[122:123] op_sel_hi:[1,0,1]\n"
    "v_cvt_pk_bf16_f32 v100, v116, v117\n"
    "v_cvt_pk_bf16_f32 v101, v118, v119\n"
    "v_cvt_pk_bf16_f32 v102, v120, v121\n"
    "v_cvt_pk_bf16_f32 v103, v122, v123\n"
    "v_pk_max_i16 v100, v100, 0\n"
    "v_pk_max_i16 v101, v101, 0\n"
    "v_pk_max_i16 v102, v102, 0\n"
    "v_pk_max_i16 v103, v103, 0\n"
    "s_nop 0\n"
    "v_permlane16_swap_b32 v100, v102\n"
    "v_permlane16_swap_b32 v101, v103\n"
    "buffer_store_dwordx4 v[100:103], v1, s[28:31], s57 offen sc1\n"
    "s_branch .Lk_w1_merge\n"
    ".Lk_w1_ch1:\n"
    "ds_read_b128 v[36:39], v56\n"
    "ds_read_b128 v[124:127], v57\n"
    "ds_read_b64_tr_b16 v[76:77], v9 offset:32768\n"
    "ds_read_b64_tr_b16 v[78:79], v9 offset:33024\n"
    "ds_read_b64_tr_b16 v[80:81], v14 offset:40960\n"
    "ds_read_b64_tr_b16 v[82:83], v14 offset:41472\n"
    "ds_read_b64_tr_b16 v[84:85], v15 offset:40960\n"
    "ds_read_b64_tr_b16 v[86:87], v15 offset:41472\n"
    "ds_read_b64_tr_b16 v[88:89], v18 offset:40960\n"
    "ds_read_b64_tr_b16 v[90:91], v18 offset:41472\n"
    "s_bfe_u32 s71, s1, 0x40010\n"
    "s_mul_i32 s71, s71, 0x30000\n"
    "s_add_u32 m0, s45, 0xc000\n"
    "s_add_u32 s71, s71, s43\n"
    "buffer_load_dwordx4 v3, s[8:11], s71 offen lds\n"
    "s_bfe_u32 s71, s0, 0x40010\n"
    "s_mul_i32 s71, s71, 0x38000\n"
    "s_add_u32 m0, s45, 0xe000\n"
    "s_add_u32 s71, s71, s46\n"
    "buffer_load_dwordx4 v4, s[12:15], s71 offen lds\n"
    "s_waitcnt lgkmcnt(9)\n"
    "v_pk_mul_f32 v[108:109], v[108:109], v[42:43] op_sel_hi:[1,0]\n"
    "v_pk_mul_f32 v[110:111], v[110:111], v[42:43] op_sel_hi:[1,0]\n"
    "v_pk_fma_f32 v[108:109], v[36:37], v[40:41], v[108:109] op_sel_hi:[1,0,1]\n"
    "v_pk_fma_f32 v[110:111], v[38:39], v[40:41], v[110:111] op_sel_hi:[1,0,1]\n"
    "v_pk_mul_f32 v[112:113], v[112:113], v[46:47] op_sel_hi:[1,0]\n"
    "v_pk_mul_f32 v[114:115], v[114:115], v[46:47] op_sel_hi:[1,0]\n"
    "v_pk_fma_f32 v[112:113], v[36:37], v[44:45], v[112:113] op_sel_hi:[1,0,1]\n"
    "v_pk_fma_f32 v[114:115], v[38:39], v[44:45], v[114:115] op_sel_hi:[1,0,1]\n"
    "v_cvt_pk_bf16_f32 v108, v108, v109\n"
    "v_cvt_pk_bf16_f32 v109, v110, v111\n"
    "v_cvt_pk_bf16_f32 v110, v112, v113\n"
    "v_cvt_pk_bf16_f32 v111, v114, v115\n"
    "s_nop 0\n"
    "v_permlane16_swap_b32 v108, v110\n"
    "v_permlane16_swap_b32 v109, v111\n"
    "buffer_store_dwordx4 v[108:111], v1, s[28:31], s2 offen sc1\n"
    "s_add_u32 s2, s2, s56\n"
    "s_waitcnt lgkmcnt(8)\n"
    "v_pk_mul_f32 v[116:117], v[116:117], v[42:43] op_sel_hi:[1,0]\n"
    "v_pk_mul_f32 v[118:119], v[118:119], v[42:43] op_sel_hi:[1,0]\n"
    "v_pk_fma_f32 v[116:117], v[124:125], v[40:41], v[116:117] op_sel_hi:[1,0,1]\n"
    "v_pk_fma_f32 v[118:119], v[126:127], v[40:41], v[118:119] op_sel_hi:[1,0,1]\n"
    "v_pk_mul_f32 v[120:121], v[120:121], v[46:47] op_sel_hi:[1,0]\n"
    "v_pk_mul_f32 v[122:123], v[122:123], v[46:47] op_sel_hi:[1,0]\n"
    "v_pk_fma_f32 v[120:121], v[124:125], v[44:45], v[120:121] op_sel_hi:[1,0,1]\n"
    "v_pk_fma_f32 v[122:123], v[126:127], v[44:45], v[122:123] op_sel_hi:[1,0,1]\n"
    "v_cvt_pk_bf16_f32 v116, v116, v117\n"
    "v_cvt_pk_bf16_f32 v117, v118, v119\n"
    "v_cvt_pk_bf16_f32 v118, v120, v121\n"
    "v_cvt_pk_bf16_f32 v119, v122, v123\n"
    "s_nop 0\n"
    "v_permlane16_swap_b32 v116, v118\n"
    "v_permlane16_swap_b32 v117, v119\n"
    "buffer_store_dwordx4 v[116:119], v1, s[28:31], s2 offen sc1\n"
    "v_cndmask_b32 v30, v21, v20, s[72:73]\n"
    "v_cndmask_b32 v21, v20, v21, s[72:73]\n"
    "v_cndmask_b32 v32, v108, v116, s[62:63]\n"
    "v_cndmask_b32 v33, v109, v117, s[62:63]\n"
    "v_cndmask_b32 v34, v110, v118, s[62:63]\n"
    "v_cndmask_b32 v35, v111, v119, s[62:63]\n"
    "ds_write_b128 v30, v[32:35] offset:32768\n"
    "s_waitcnt lgkmcnt(1)\n"
    "v_mfma_f32_16x16x32_bf16 v[48:51], v[76:79], v[128:131], 0\n"
    "ds_read_b64_tr_b16 v[92:93], v9 offset:34816\n"
    "ds_read_b64_tr_b16 v[94:95], v9 offset:35072\n"
    "s_bfe_u32 s71, s0, 0x40014\n"
    "v_mfma_f32_16x16x32_bf16 v[52:55], v[76:79], v[132:135], 0\n"
    "ds_read_b64_tr_b16 v[96:97], v14 offset:45056\n"
    "ds_read_b64_tr_b16 v[98:99], v14 offset:45568\n"
    "s_mul_i32 s71, s71, 0x38000\n"
    "v_mfma_f32_16x16x32_bf16 v[56:59], v[80:83], v[128:131], 0\n"
    "ds_read_b64_tr_b16 v[100:101], v15 offset:45056\n"
    "ds_read_b64_tr_b16 v[102:103], v15 offset:45568\n"
    "s_add_u32 m0, s45, 0x10000\n"
    "v_mfma_f32_16x16x32_bf16 v[60:63], v[80:83], v[132:135], 0\n"
    "ds_read_b64_tr_b16 v[104:105], v18 offset:45056\n"
    "ds_read_b64_tr_b16 v[106:107], v18 offset:45568\n"
    "s_add_u32 s71, s71, s46\n"
    "v_mfma_f32_16x16x32_bf16 v[64:67], v[84:87], v[128:131], 0\n"
    "buffer_load_dwordx4 v4, s[12:15], s71 offen lds\n"
    "s_cmp_ge_u32 s34, 2\n"
    "s_cselect_b32 s66, 0x80000, 0\n"
    "s_cselect_b32 s67, 0, 0x40000000\n"
    "s_cselect_b32 s68, 0x1000, 0\n"
    "s_sub_u32 s53, s52, s66\n"
    "v_mfma_f32_16x16x32_bf16 v[68:71], v[84:87], v[132:135], 0\n"
    "v_add_u32 v126, s67, v1\n"
    "s_cmp_ge_u32 s34, 1\n"
    "s_cselect_b32 s66, 0x40000, 0\n"
    "s_cselect_b32 s67, 0, 0x40000000\n"
    "s_cselect_b32 s69, 0x800, 0\n"
    "s_sub_u32 s54, s52, s66\n"
    "v_mfma_f32_16x16x32_bf16 v[72:75], v[88:91], v[24:27], 0\n"
    "v_add_u32 v127, s67, v1\n"
    "s_lshl_b32 s66, s39, 3\n"
    "s_waitcnt lgkmcnt(0)\n"
    "v_mfma_f32_16x16x32_bf16 v[48:51], v[92:95], v[136:139], v[48:51]\n"
    "ds_read_b64_tr_b16 v[76:77], v9 offset:36864\n"
    "ds_read_b64_tr_b16 v[78:79], v9 offset:37120\n"
    "s_bfe_u32 s71, s1, 0x40018\n"
    "v_mfma_f32_16x16x32_bf16 v[52:55], v[92:95], v[140:143], v[52:55]\n"
    "ds_read_b64_tr_b16 v[80:81], v14\n"
    "ds_read_b64_tr_b16 v[82:83], v14 offset:512\n"
    "s_mul_i32 s71, s71, 0x30000\n"
    "v_mfma_f32_16x16x32_bf16 v[56:59], v[96:99], v[136:139], v[56:59]\n"
    "ds_read_b64_tr_b16 v[84:85], v15\n"
    "ds_read_b64_tr_b16 v[86:87], v15 offset:512\n"
    "s_add_u32 m0, s45, 0x12000\n"
    "v_mfma_f32_16x16x32_bf16 v[60:63], v[96:99], v[140:143], v[60:63]\n"
    "ds_read_b64_tr_b16 v[88:89], v18\n"
    "ds_read_b64_tr_b16 v[90:91], v18 offset:512\n"
    "s_add_u32 s71, s71, s43\n"
    "v_mfma_f32_16x16x32_bf16 v[64:67], v[100:103], v[136:139], v[64:67]\n"
    "buffer_load_dwordx4 v3, s[8:11], s71 offen lds\n"
    "s_sub_u32 s66, s49, s66\n"
    "v_and_b32 v37, 1, v0\n"
    "v_mfma_f32_16x16x32_bf16 v[68:71], v[100:103], v[140:143], v[68:71]\n"
    "v_lshl_add_u32 v125, v37, 3, s66\n"
    "s_mov_b64 exec, 3\n"
    "v_subrev_u32 v125, s68, v125\n"
    "s_mov_b64 exec, 12\n"
    "v_subrev_u32 v125, s69, v125\n"
    "s_mov_b64 exec, -1\n"
    "s_lshl_b32 s66, s39, 10\n"
    "v_mfma_f32_16x16x32_bf16 v[72:75], v[104:107], v[24:27], v[72:75]\n"
    "s_sub_u32 s42, s52, s66\n"
    "s_add_u32 s42, s42, 0x400\n"
    "s_waitcnt lgkmcnt(0)\n"
    "v_mfma_f32_16x16x32_bf16 v[48:51], v[76:79], v[144:147], v[48:51]\n"
    "ds_read_b64_tr_b16 v[92:93], v9 offset:38912\n"
    "ds_read_b64_tr_b16 v[94:95], v9 offset:39168\n"
    "s_bfe_u32 s71, s0, 0x40018\n"
    "v_mfma_f32_16x16x32_bf16 v[52:55], v[76:79], v[148:151], v[52:55]\n"
    "ds_read_b64_tr_b16 v[96:97], v14 offset:4096\n"
    "ds_read_b64_tr_b16 v[98:99], v14 offset:4608\n"
    "s_mul_i32 s71, s71, 0x38000\n"
    "v_mfma_f32_16x16x32_bf16 v[56:59], v[80:83], v[144:147], v[56:59]\n"
    "ds_read_b64_tr_b16 v[100:101], v15 offset:4096\n"
    "ds_read_b64_tr_b16 v[102:103], v15 offset:4608\n"
    "s_add_u32 m0, s45, 0x14000\n"
    "v_mfma_f32_16x16x32_bf16 v[60:63], v[80:83], v[148:151], v[60:63]\n"
    "ds_read_b64_tr_b16 v[104:105], v18 offset:4096\n"
    "ds_read_b64_tr_b16 v[106:107], v18 offset:4608\n"
    "s_add_u32 s71, s71, s46\n"
    "v_mfma_f32_16x16x32_bf16 v[64:67], v[84:87], v[144:147], v[64:67]\n"
    "buffer_load_dwordx4 v4, s[12:15], s71 offen lds\n"
    "s_min_u32 s65, s47, 8\n"
    "v_and_b32 v2, 7, v0\n"
    "v_mfma_f32_16x16x32_bf16 v[68:71], v[84:87], v[148:151], v[68:71]\n"
    "v_lshlrev_b32 v2, 2, v2\n"
    "s_and_b32 s66, s32, 7\n"
    "s_lshl_b32 s66, s66, 2\n"
    "v_mfma_f32_16x16x32_bf16 v[72:75], v[88:91], v[24:27], v[72:75]\n"
    "s_lshr_b32 s67, s32, 6\n"
    "s_add_u32 s66, s66, s67\n"
    "s_waitcnt vmcnt(7) lgkmcnt(0)\n"
    "s_barrier\n"
    "v_mfma_f32_16x16x32_bf16 v[48:51], v[92:95], v[152:155], v[48:51]\n"
    "ds_read_b64_tr_b16 v[76:77], v9 offset:8192\n"
    "ds_read_b64_tr_b16 v[78:79], v9 offset:8448\n"
    "s_waitcnt vmcnt(3)\n"
    "s_add_u32 s71, s49, 0x8000\n"
    "s_mov_b64 exec, 1\n"
    "buffer_store_dword v22, off, s[28:31], s71 sc1\n"
    "s_mov_b64 exec, -1\n"
    "v_mfma_f32_16x16x32_bf16 v[52:55], v[92:95], v[156:159], v[52:55]\n"
    "ds_read_b64_tr_b16 v[80:81], v14 offset:16384\n"
    "ds_read_b64_tr_b16 v[82:83], v14 offset:16896\n"
    "s_bfe_u32 s71, s0, 0x4001c\n"
    "v_mfma_f32_16x16x32_bf16 v[56:59], v[96:99], v[152:155], v[56:59]\n"
    "ds_read_b64_tr_b16 v[84:85], v15 offset:16384\n"
    "ds_read_b64_tr_b16 v[86:87], v15 offset:16896\n"
    "s_mul_i32 s71, s71, 0x38000\n"
    "v_mfma_f32_16x16x32_bf16 v[60:63], v[96:99], v[156:159], v[60:63]\n"
    "ds_read_b64_tr_b16 v[88:89], v18 offset:16384\n"
    "ds_read_b64_tr_b16 v[90:91], v18 offset:16896\n"
    "s_add_u32 m0, s45, 0x16000\n"
    "v_mfma_f32_16x16x32_bf16 v[64:67], v[100:103], v[152:155], v[64:67]\n"
    "s_add_u32 s71, s71, s46\n"
    "buffer_load_dwordx4 v4, s[12:15], s71 offen lds\n"
    "s_bfe_u32 s67, s32, 0x30003\n"
    "v_mfma_f32_16x16x32_bf16 v[68:71], v[100:103], v[156:159], v[68:71]\n"
    "s_lshl_b32 s67, s67, 7\n"
    "s_lshl_b32 s69, s37, 13\n"
    "s_add_u32 s69, s69, s67\n"
    "v_mfma_f32_16x16x32_bf16 v[72:75], v[104:107], v[24:27], v[72:75]\n"
    "s_mul_i32 s59, s36, 0x70000\n"
    "s_add_u32 s59, s59, s69\n"
    "s_waitcnt lgkmcnt(0)\n"
    "v_mfma_f32_16x16x32_bf16 v[48:51], v[76:79], v[160:163], v[48:51]\n"
    "ds_read_b64_tr_b16 v[92:93], v9 offset:10240\n"
    "ds_read_b64_tr_b16 v[94:95], v9 offset:10496\n"
    "s_bfe_u32 s71, s0, 0x40000\n"
    "v_mfma_f32_16x16x32_bf16 v[52:55], v[76:79], v[164:167], v[52:55]\n"
    "ds_read_b64_tr_b16 v[96:97], v14 offset:20480\n"
    "ds_read_b64_tr_b16 v[98:99], v14 offset:20992\n"
    "s_mul_i32 s71, s71, 0x38000\n"
    "v_mfma_f32_16x16x32_bf16 v[56:59], v[80:83], v[160:163], v[56:59]\n"
    "ds_read_b64_tr_b16 v[100:101], v15 offset:20480\n"
    "ds_read_b64_tr_b16 v[102:103], v15 offset:20992\n"
    "s_add_u32 m0, s45, 0x18000\n"
    "v_mfma_f32_16x16x32_bf16 v[60:63], v[80:83], v[164:167], v[60:63]\n"
    "ds_read_b64_tr_b16 v[104:105], v18 offset:20480\n"
    "ds_read_b64_tr_b16 v[106:107], v18 offset:20992\n"
    "s_add_u32 s71, s71, s3\n"
    "v_mfma_f32_16x16x32_bf16 v[64:67], v[84:87], v[160:163], v[64:67]\n"
    "buffer_load_dwordx4 v10, s[12:15], s71 offen lds\n"
    "s_lshl_b32 s60, s36, 17\n"
    "s_add_u32 s60, s60, s69\n"
    "v_mfma_f32_16x16x32_bf16 v[68:71], v[84:87], v[164:167], v[68:71]\n"
    "s_lshl_b32 s61, s36, 11\n"
    "s_lshl_b32 s69, s37, 10\n"
    "s_add_u32 s61, s61, s69\n"
    "v_mfma_f32_16x16x32_bf16 v[72:75], v[88:91], v[24:27], v[72:75]\n"
    "s_lshl_b32 s68, s66, 1\n"
    "s_add_u32 s68, s68, s37\n"
    "s_waitcnt lgkmcnt(0)\n"
    "v_mfma_f32_16x16x32_bf16 v[48:51], v[92:95], v[168:171], v[48:51]\n"
    "ds_read_b64_tr_b16 v[76:77], v9 offset:12288\n"
    "ds_read_b64_tr_b16 v[78:79], v9 offset:12544\n"
    "s_bfe_u32 s71, s0, 0x40004\n"
    "v_mfma_f32_16x16x32_bf16 v[52:55], v[92:95], v[172:175], v[52:55]\n"
    "ds_read_b64_tr_b16 v[80:81], v14 offset:24576\n"
    "ds_read_b64_tr_b16 v[82:83], v14 offset:25088\n"
    "s_mul_i32 s71, s71, 0x38000\n"
    "v_mfma_f32_16x16x32_bf16 v[56:59], v[96:99], v[168:171], v[56:59]\n"
    "ds_read_b64_tr_b16 v[84:85], v15 offset:24576\n"
    "ds_read_b64_tr_b16 v[86:87], v15 offset:25088\n"
    "s_add_u32 m0, s45, 0x1a000\n"
    "v_mfma_f32_16x16x32_bf16 v[60:63], v[96:99], v[172:175], v[60:63]\n"
    "ds_read_b64_tr_b16 v[88:89], v18 offset:24576\n"
    "ds_read_b64_tr_b16 v[90:91], v18 offset:25088\n"
    "s_add_u32 s71, s71, s3\n"
    "v_mfma_f32_16x16x32_bf16 v[64:67], v[100:103], v[168:171], v[64:67]\n"
    "buffer_load_dwordx4 v10, s[12:15], s71 offen lds\n"
    "s_mul_i32 s58, s68, 0x90\n"
    "s_mul_i32 s69, s36, 28\n"
    "v_mfma_f32_16x16x32_bf16 v[68:71], v[100:103], v[172:175], v[68:71]\n"
    "s_add_u32 s58, s58, s69\n"
    "s_lshl_b32 s58, s58, 10\n"
    "s_add_u32 s58, s58, 0x600000\n"
    "v_mfma_f32_16x16x32_bf16 v[72:75], v[104:107], v[24:27], v[72:75]\n"
    "s_lshl_b32 s62, s68, 2\n"
    "s_add_u32 s62, s62, s36\n"
    "s_waitcnt lgkmcnt(0)\n"
    "v_mfma_f32_16x16x32_bf16 v[48:51], v[76:79], v[176:179], v[48:51]\n"
    "ds_read_b64_tr_b16 v[92:93], v9 offset:14336\n"
    "ds_read_b64_tr_b16 v[94:95], v9 offset:14592\n"
    "s_bfe_u32 s71, s0, 0x40008\n"
    "v_mfma_f32_16x16x32_bf16 v[52:55], v[76:79], v[180:183], v[52:55]\n"
    "ds_read_b64_tr_b16 v[96:97], v14 offset:28672\n"
    "ds_read_b64_tr_b16 v[98:99], v14 offset:29184\n"
    "s_mul_i32 s71, s71, 0x38000\n"
    "v_mfma_f32_16x16x32_bf16 v[56:59], v[80:83], v[176:179], v[56:59]\n"
    "ds_read_b64_tr_b16 v[100:101], v15 offset:28672\n"
    "ds_read_b64_tr_b16 v[102:103], v15 offset:29184\n"
    "s_add_u32 m0, s45, 0x8000\n"
    "v_mfma_f32_16x16x32_bf16 v[60:63], v[80:83], v[180:183], v[60:63]\n"
    "ds_read_b64_tr_b16 v[104:105], v18 offset:28672\n"
    "ds_read_b64_tr_b16 v[106:107], v18 offset:29184\n"
    "s_add_u32 s71, s71, s3\n"
    "v_mfma_f32_16x16x32_bf16 v[64:67], v[84:87], v[176:179], v[64:67]\n"
    "buffer_load_dwordx4 v10, s[12:15], s71 offen lds\n"
    "s_lshl_b32 s62, s62, 7\n"
    "s_lshl_b32 s63, s66, 3\n"
    "v_mfma_f32_16x16x32_bf16 v[68:71], v[84:87], v[180:183], v[68:71]\n"
    "s_add_u32 s63, s63, s33\n"
    "s_lshl_b32 s63, s63, 13\n"
    "s_add_u32 s63, s63, s67\n"
    "v_mfma_f32_16x16x32_bf16 v[72:75], v[88:91], v[24:27], v[72:75]\n"
    "v_bfe_u32 v37, v0, 2, 2\n"
    "v_lshrrev_b32 v38, 5, v0\n"
    "s_waitcnt vmcnt(7) lgkmcnt(0)\n"
    "s_barrier\n"
    "v_mfma_f32_16x16x32_bf16 v[48:51], v[92:95], v[184:187], v[48:51]\n"
    "ds_read_b64_tr_b16 v[76:77], v9 offset:49152\n"
    "ds_read_b64_tr_b16 v[78:79], v9 offset:49408\n"
    "s_bfe_u32 s71, s0, 0x4000c\n"
    "v_mfma_f32_16x16x32_bf16 v[52:55], v[92:95], v[188:191], v[52:55]\n"
    "ds_read_b64_tr_b16 v[80:81], v14 offset:57344\n"
    "ds_read_b64_tr_b16 v[82:83], v14 offset:57856\n"
    "s_mul_i32 s71, s71, 0x38000\n"
    "v_mfma_f32_16x16x32_bf16 v[56:59], v[96:99], v[184:187], v[56:59]\n"
    "ds_read_b64_tr_b16 v[84:85], v15 offset:57344\n"
    "ds_read_b64_tr_b16 v[86:87], v15 offset:57856\n"
    "s_add_u32 m0, s45, 0xa000\n"
    "v_mfma_f32_16x16x32_bf16 v[60:63], v[96:99], v[188:191], v[60:63]\n"
    "ds_read_b64_tr_b16 v[88:89], v18 offset:57344\n"
    "ds_read_b64_tr_b16 v[90:91], v18 offset:57856\n"
    "s_add_u32 s71, s71, s3\n"
    "v_mfma_f32_16x16x32_bf16 v[64:67], v[100:103], v[184:187], v[64:67]\n"
    "buffer_load_dwordx4 v10, s[12:15], s71 offen lds\n"
    "v_lshl_add_u32 v38, v38, 3, v37\n"
    "v_lshlrev_b32 v38, 7, v38\n"
    "v_mfma_f32_16x16x32_bf16 v[68:71], v[100:103], v[188:191], v[68:71]\n"
    "v_bfe_u32 v39, v0, 4, 1\n"
    "v_lshl_add_u32 v38, v39, 5, v38\n"
    "v_and_b32 v39, 3, v0\n"
    "v_mfma_f32_16x16x32_bf16 v[72:75], v[104:107], v[24:27], v[72:75]\n"
    "v_lshl_add_u32 v38, v39, 3, v38\n"
    "s_lshl_b32 s69, s36, 11\n"
    "s_waitcnt lgkmcnt(0)\n"
    "v_mfma_f32_16x16x32_bf16 v[48:51], v[76:79], v[192:195], v[48:51]\n"
    "ds_read_b64_tr_b16 v[92:93], v9 offset:51200\n"
    "ds_read_b64_tr_b16 v[94:95], v9 offset:51456\n"
    "s_bfe_u32 s71, s0, 0x40010\n"
    "v_mfma_f32_16x16x32_bf16 v[52:55], v[76:79], v[196:199], v[52:55]\n"
    "ds_read_b64_tr_b16 v[96:97], v14 offset:61440\n"
    "ds_read_b64_tr_b16 v[98:99], v14 offset:61952\n"
    "s_mul_i32 s71, s71, 0x38000\n"
    "v_mfma_f32_16x16x32_bf16 v[56:59], v[80:83], v[192:195], v[56:59]\n"
    "ds_read_b64_tr_b16 v[100:101], v15 offset:61440\n"
    "ds_read_b64_tr_b16 v[102:103], v15 offset:61952\n"
    "s_mov_b32 m0, s45\n"
    "v_mfma_f32_16x16x32_bf16 v[60:63], v[80:83], v[196:199], v[60:63]\n"
    "ds_read_b64_tr_b16 v[104:105], v18 offset:61440\n"
    "ds_read_b64_tr_b16 v[106:107], v18 offset:61952\n"
    "s_add_u32 s71, s71, s3\n"
    "v_mfma_f32_16x16x32_bf16 v[64:67], v[84:87], v[192:195], v[64:67]\n"
    "buffer_load_dwordx4 v10, s[12:15], s71 offen lds\n"
    "v_add_u32 v38, s69, v38\n"
    "v_lshrrev_b32 v37, 1, v37\n"
    "v_mfma_f32_16x16x32_bf16 v[68:71], v[84:87], v[196:199], v[68:71]\n"
    "v_lshl_add_u32 v6, v37, 6, v38\n"
    "v_xor_b32 v7, 64, v6\n"
    "v_mfma_f32_16x16x32_bf16 v[72:75], v[88:91], v[24:27], v[72:75]\n"
    "s_waitcnt lgkmcnt(0)\n"
    "v_mfma_f32_16x16x32_bf16 v[48:51], v[92:95], v[200:203], v[48:51]\n"
    "ds_read_b64_tr_b16 v[76:77], v9 offset:53248\n"
    "ds_read_b64_tr_b16 v[78:79], v9 offset:53504\n"
    "v_mfma_f32_16x16x32_bf16 v[52:55], v[92:95], v[204:207], v[52:55]\n"
    "v_add_u32 v14, 0x10000, v14\n"
    "ds_read_b64_tr_b16 v[80:81], v14\n"
    "v_mfma_f32_16x16x32_bf16 v[56:59], v[96:99], v[200:203], v[56:59]\n"
    "ds_read_b64_tr_b16 v[82:83], v14 offset:512\n"
    "v_add_u32 v15, 0x10000, v15\n"
    "v_mfma_f32_16x16x32_bf16 v[60:63], v[96:99], v[204:207], v[60:63]\n"
    "ds_read_b64_tr_b16 v[84:85], v15\n"
    "ds_read_b64_tr_b16 v[86:87], v15 offset:512\n"
    "v_mfma_f32_16x16x32_bf16 v[64:67], v[100:103], v[200:203], v[64:67]\n"
    "v_add_u32 v18, 0x10000, v18\n"
    "ds_read_b64_tr_b16 v[88:89], v18\n"
    "s_bfe_u32 s71, s0, 0x40014\n"
    "v_mfma_f32_16x16x32_bf16 v[68:71], v[100:103], v[204:207], v[68:71]\n"
    "ds_read_b64_tr_b16 v[90:91], v18 offset:512\n"
    "s_mul_i32 s71, s71, 0x38000\n"
    "s_add_u32 m0, s45, 0x2000\n"
    "v_mfma_f32_16x16x32_bf16 v[72:75], v[104:107], v[24:27], v[72:75]\n"
    "s_add_u32 s71, s71, s3\n"
    "buffer_load_dwordx4 v10, s[12:15], s71 offen lds\n"
    "s_waitcnt lgkmcnt(0)\n"
    "v_mfma_f32_16x16x32_bf16 v[48:51], v[76:79], v[208:211], v[48:51]\n"
    "ds_read_b64_tr_b16 v[92:93], v9 offset:55296\n"
    "ds_read_b64_tr_b16 v[94:95], v9 offset:55552\n"
    "v_mfma_f32_16x16x32_bf16 v[52:55], v[76:79], v[212:215], v[52:55]\n"
    "ds_read_b64_tr_b16 v[96:97], v14 offset:4096\n"
    "ds_read_b64_tr_b16 v[98:99], v14 offset:4608\n"
    "v_mfma_f32_16x16x32_bf16 v[56:59], v[80:83], v[208:211], v[56:59]\n"
    "ds_read_b64_tr_b16 v[100:101], v15 offset:4096\n"
    "ds_read_b64_tr_b16 v[102:103], v15 offset:4608\n"
    "v_mfma_f32_16x16x32_bf16 v[60:63], v[80:83], v[212:215], v[60:63]\n"
    "ds_read_b64_tr_b16 v[104:105], v18 offset:4096\n"
    "ds_read_b64_tr_b16 v[106:107], v18 offset:4608\n"
    "v_mfma_f32_16x16x32_bf16 v[64:67], v[84:87], v[208:211], v[64:67]\n"
    "s_bfe_u32 s71, s0, 0x40018\n"
    "s_mul_i32 s71, s71, 0x38000\n"
    "v_mfma_f32_16x16x32_bf16 v[68:71], v[84:87], v[212:215], v[68:71]\n"
    "s_add_u32 m0, s45, 0x4000\n"
    "s_add_u32 s71, s71, s3\n"
    "v_mfma_f32_16x16x32_bf16 v[72:75], v[88:91], v[24:27], v[72:75]\n"
    "buffer_load_dwordx4 v10, s[12:15], s71 offen lds\n"
    "s_waitcnt vmcnt(7) lgkmcnt(0)\n"
    "s_barrier\n"
    "v_mfma_f32_16x16x32_bf16 v[48:51], v[92:95], v[216:219], v[48:51]\n"
    "v_add_u32 v9, 0x10000, v9\n"
    "ds_read_b64_tr_b16 v[76:77], v9 offset:8192\n"
    "v_mfma_f32_16x16x32_bf16 v[52:55], v[92:95], v[220:223], v[52:55]\n"
    "ds_read_b64_tr_b16 v[78:79], v9 offset:8448\n"
    "ds_read_b64_tr_b16 v[80:81], v14 offset:16384\n"
    "v_mfma_f32_16x16x32_bf16 v[56:59], v[96:99], v[216:219], v[56:59]\n"
    "ds_read_b64_tr_b16 v[82:83], v14 offset:16896\n"
    "ds_read_b64_tr_b16 v[84:85], v15 offset:16384\n"
    "v_mfma_f32_16x16x32_bf16 v[60:63], v[96:99], v[220:223], v[60:63]\n"
    "ds_read_b64_tr_b16 v[86:87], v15 offset:16896\n"
    "ds_read_b64_tr_b16 v[88:89], v18 offset:16384\n"
    "v_mfma_f32_16x16x32_bf16 v[64:67], v[100:103], v[216:219], v[64:67]\n"
    "ds_read_b64_tr_b16 v[90:91], v18 offset:16896\n"
    "s_mov_b32 s71, 0x8000\n"
    "buffer_load_dword v124, v125, s[28:31], s71 offen sc1\n"
    "v_mfma_f32_16x16x32_bf16 v[68:71], v[100:103], v[220:223], v[68:71]\n"
    "s_bfe_u32 s71, s0, 0x4001c\n"
    "s_mul_i32 s71, s71, 0x38000\n"
    "s_add_u32 m0, s45, 0x6000\n"
    "v_mfma_f32_16x16x32_bf16 v[72:75], v[104:107], v[24:27], v[72:75]\n"
    "s_add_u32 s71, s71, s3\n"
    "buffer_load_dwordx4 v10, s[12:15], s71 offen lds\n"
    "s_waitcnt lgkmcnt(0)\n"
    "v_mfma_f32_16x16x32_bf16 v[48:51], v[76:79], v[224:227], v[48:51]\n"
    "ds_read_b64_tr_b16 v[92:93], v9 offset:10240\n"
    "ds_read_b64_tr_b16 v[94:95], v9 offset:10496\n"
    "v_mfma_f32_16x16x32_bf16 v[52:55], v[76:79], v[228:231], v[52:55]\n"
    "ds_read_b64_tr_b16 v[96:97], v14 offset:20480\n"
    "ds_read_b64_tr_b16 v[98:99], v14 offset:20992\n"
    "v_mfma_f32_16x16x32_bf16 v[56:59], v[80:83], v[224:227], v[56:59]\n"
    "ds_read_b64_tr_b16 v[100:101], v15 offset:20480\n"
    "ds_read_b64_tr_b16 v[102:103], v15 offset:20992\n"
    "v_mfma_f32_16x16x32_bf16 v[60:63], v[80:83], v[228:231], v[60:63]\n"
    "ds_read_b64_tr_b16 v[104:105], v18 offset:20480\n"
    "ds_read_b64_tr_b16 v[106:107], v18 offset:20992\n"
    "v_mfma_f32_16x16x32_bf16 v[64:67], v[84:87], v[224:227], v[64:67]\n"
    "s_add_u32 m0, s61, 0xc000\n"
    "s_mov_b32 s71, s59\n"
    "v_mfma_f32_16x16x32_bf16 v[68:71], v[84:87], v[228:231], v[68:71]\n"
    "buffer_load_dwordx4 v5, s[16:19], s71 offen lds\n"
    "v_mfma_f32_16x16x32_bf16 v[72:75], v[88:91], v[24:27], v[72:75]\n"
    "s_waitcnt lgkmcnt(0)\n"
    "v_mfma_f32_16x16x32_bf16 v[48:51], v[92:95], v[232:235], v[48:51]\n"
    "ds_read_b64_tr_b16 v[76:77], v9 offset:12288\n"
    "ds_read_b64_tr_b16 v[78:79], v9 offset:12544\n"
    "v_mfma_f32_16x16x32_bf16 v[52:55], v[92:95], v[236:239], v[52:55]\n"
    "ds_read_b64_tr_b16 v[80:81], v14 offset:24576\n"
    "ds_read_b64_tr_b16 v[82:83], v14 offset:25088\n"
    "v_mfma_f32_16x16x32_bf16 v[56:59], v[96:99], v[232:235], v[56:59]\n"
    "ds_read_b64_tr_b16 v[84:85], v15 offset:24576\n"
    "ds_read_b64_tr_b16 v[86:87], v15 offset:25088\n"
    "v_mfma_f32_16x16x32_bf16 v[60:63], v[96:99], v[236:239], v[60:63]\n"
    "ds_read_b64_tr_b16 v[88:89], v18 offset:24576\n"
    "ds_read_b64_tr_b16 v[90:91], v18 offset:25088\n"
    "v_mfma_f32_16x16x32_bf16 v[64:67], v[100:103], v[232:235], v[64:67]\n"
    "s_waitcnt vmcnt(2)\n"
    "v_cmp_ne_u32 vcc, v22, v124\n"
    "s_cbranch_vccnz .Lk_spin_5\n"
    ".Lk_ok_6:\n"
    "buffer_load_dwordx4 v[108:111], v126, s[28:31], s53 offen sc1\n"
    "v_mfma_f32_16x16x32_bf16 v[68:71], v[100:103], v[236:239], v[68:71]\n"
    "buffer_load_dwordx4 v[112:115], v126, s[28:31], s53 offen offset:1024 sc1\n"
    "s_add_u32 m0, s61, 0xe000\n"
    "v_mfma_f32_16x16x32_bf16 v[72:75], v[104:107], v[24:27], v[72:75]\n"
    "s_add_u32 s71, s59, 0x4000\n"
    "buffer_load_dwordx4 v5, s[16:19], s71 offen lds\n"
    "s_waitcnt lgkmcnt(0)\n"
    "v_mfma_f32_16x16x32_bf16 v[48:51], v[76:79], v[240:243], v[48:51]\n"
    "ds_read_b64_tr_b16 v[92:93], v9 offset:14336\n"
    "ds_read_b64_tr_b16 v[94:95], v9 offset:14592\n"
    "v_mfma_f32_16x16x32_bf16 v[52:55], v[76:79], v[244:247], v[52:55]\n"
    "ds_read_b64_tr_b16 v[96:97], v14 offset:28672\n"
    "ds_read_b64_tr_b16 v[98:99], v14 offset:29184\n"
    "v_mfma_f32_16x16x32_bf16 v[56:59], v[80:83], v[240:243], v[56:59]\n"
    "ds_read_b64_tr_b16 v[100:101], v15 offset:28672\n"
    "ds_read_b64_tr_b16 v[102:103], v15 offset:29184\n"
    "v_mfma_f32_16x16x32_bf16 v[60:63], v[80:83], v[244:247], v[60:63]\n"
    "ds_read_b64_tr_b16 v[104:105], v18 offset:28672\n"
    "ds_read_b64_tr_b16 v[106:107], v18 offset:29184\n"
    "v_mfma_f32_16x16x32_bf16 v[64:67], v[84:87], v[240:243], v[64:67]\n"
    "buffer_load_dwordx4 v[116:119], v127, s[28:31], s54 offen sc1\n"
    "buffer_load_dwordx4 v[120:123], v127, s[28:31], s54 offen offset:1024 sc1\n"
    "v_mfma_f32_16x16x32_bf16 v[68:71], v[84:87], v[244:247], v[68:71]\n"
    "s_add_u32 m0, s61, 0x10000\n"
    "s_add_u32 s71, s59, 0x8000\n"
    "v_mfma_f32_16x16x32_bf16 v[72:75], v[88:91], v[24:27], v[72:75]\n"
    "buffer_load_dwordx4 v5, s[16:19], s71 offen lds\n"
    "s_waitcnt lgkmcnt(0)\n"
    "v_mfma_f32_16x16x32_bf16 v[72:75], v[104:107], v[24:27], v[72:75]\n"
    "buffer_load_dwordx4 v[124:127], v1, s[28:31], s42 offen sc1\n"
    "v_mfma_f32_16x16x32_bf16 v[48:51], v[92:95], v[248:251], v[48:51]\n"
    "v_mfma_f32_16x16x32_bf16 v[52:55], v[92:95], v[252:255], v[52:55]\n"
    "v_mfma_f32_16x16x32_bf16 v[56:59], v[96:99], v[248:251], v[56:59]\n"
    "v_mfma_f32_16x16x32_bf16 v[60:63], v[96:99], v[252:255], v[60:63]\n"
    "v_mfma_f32_16x16x32_bf16 v[64:67], v[100:103], v[248:251], v[64:67]\n"
    "v_mfma_f32_16x16x32_bf16 v[68:71], v[100:103], v[252:255], v[68:71]\n"
    "s_mov_b32 exec_lo, 0x10001\n"
    "s_mov_b32 exec_hi, 0x10001\n"
    "ds_write_b128 v11, v[72:75] offset:512\n"
    "s_mov_b64 exec, -1\n"
    "s_waitcnt lgkmcnt(0)\n"
    "s_barrier\n"
    "ds_read_b128 v[36:39], v12 offset:768\n"
    "v_add_u32 v16, 0x10000, v16\n"
    "ds_read_b64_tr_b16 v[76:77], v16 offset:32768\n"
    "ds_read_b64_tr_b16 v[78:79], v16 offset:33280\n"
    "ds_read_b64_tr_b16 v[80:81], v18 offset:32768\n"
    "ds_read_b64_tr_b16 v[82:83], v18 offset:33280\n"
    "ds_read_b64_tr_b16 v[84:85], v16 offset:36864\n"
    "ds_read_b64_tr_b16 v[86:87], v16 offset:37376\n"
    "ds_read_b64_tr_b16 v[88:89], v18 offset:36864\n"
    "ds_read_b64_tr_b16 v[90:91], v18 offset:37376\n"
    "s_add_u32 m0, s61, 0x12000\n"
    "s_add_u32 s71, s59, 0xc000\n"
    "buffer_load_dwordx4 v5, s[16:19], s71 offen lds\n"
    "s_waitcnt lgkmcnt(8)\n"
    "v_pk_mul_f32 v[56:57], v[56:57], v[42:43] op_sel_hi:[1,0]\n"
    "v_pk_mul_f32 v[58:59], v[58:59], v[42:43] op_sel_hi:[1,0]\n"
    "v_pk_fma_f32 v[56:57], v[36:37], v[40:41], v[56:57] op_sel_hi:[1,0,1]\n"
    "v_pk_fma_f32 v[58:59], v[38:39], v[40:41], v[58:59] op_sel_hi:[1,0,1]\n"
    "v_pk_mul_f32 v[60:61], v[60:61], v[46:47] op_sel_hi:[1,0]\n"
    "v_pk_mul_f32 v[62:63], v[62:63], v[46:47] op_sel_hi:[1,0]\n"
    "v_pk_fma_f32 v[60:61], v[36:37], v[44:45], v[60:61] op_sel_hi:[1,0,1]\n"
    "v_pk_fma_f32 v[62:63], v[38:39], v[44:45], v[62:63] op_sel_hi:[1,0,1]\n"
    "ds_read_b128 v[36:39], v12 offset:896\n"
    "v_cvt_pk_bf16_f32 v56, v56, v57\n"
    "v_cvt_pk_bf16_f32 v57, v58, v59\n"
    "v_cvt_pk_bf16_f32 v58, v60, v61\n"
    "v_cvt_pk_bf16_f32 v59, v62, v63\n"
    "v_pk_max_i16 v56, v56, 0\n"
    "v_pk_max_i16 v57, v57, 0\n"
    "v_pk_max_i16 v58, v58, 0\n"
    "v_pk_max_i16 v59, v59, 0\n"
    "s_nop 0\n"
    "v_permlane16_swap_b32 v56, v58\n"
    "v_permlane16_swap_b32 v57, v59\n"
    "buffer_store_dwordx4 v[56:59], v1, s[28:31], s50 offen sc1\n"
    "s_waitcnt lgkmcnt(0)\n"
    "v_pk_mul_f32 v[64:65], v[64:65], v[42:43] op_sel_hi:[1,0]\n"
    "v_pk_mul_f32 v[66:67], v[66:67], v[42:43] op_sel_hi:[1,0]\n"
    "v_pk_fma_f32 v[64:65], v[36:37], v[40:41], v[64:65] op_sel_hi:[1,0,1]\n"
    "v_pk_fma_f32 v[66:67], v[38:39], v[40:41], v[66:67] op_sel_hi:[1,0,1]\n"
    "v_pk_mul_f32 v[68:69], v[68:69], v[46:47] op_sel_hi:[1,0]\n"
    "v_pk_mul_f32 v[70:71], v[70:71], v[46:47] op_sel_hi:[1,0]\n"
    "v_pk_fma_f32 v[68:69], v[36:37], v[44:45], v[68:69] op_sel_hi:[1,0,1]\n"
    "v_pk_fma_f32 v[70:71], v[38:39], v[44:45], v[70:71] op_sel_hi:[1,0,1]\n"
    "ds_read_b128 v[36:39], v12 offset:512\n"
    "v_cvt_pk_bf16_f32 v64, v64, v65\n"
    "v_cvt_pk_bf16_f32 v65, v66, v67\n"
    "v_cvt_pk_bf16_f32 v66, v68, v69\n"
    "v_cvt_pk_bf16_f32 v67, v70, v71\n"
    "v_pk_max_i16 v64, v64, 0\n"
    "v_pk_max_i16 v65, v65, 0\n"
    "v_pk_max_i16 v66, v66, 0\n"
    "v_pk_max_i16 v67, v67, 0\n"
    "s_nop 0\n"
    "v_permlane16_swap_b32 v64, v66\n"
    "v_permlane16_swap_b32 v65, v67\n"
    "buffer_store_dwordx4 v[64:67], v1, s[28:31], s50 offen offset:2048 sc1\n"
    "v_mul_f32 v60, 0x3eb8aa3b, v42\n"
    "v_mul_f32 v62, 0x3eb8aa3b, v40\n"
    "v_mul_f32 v68, 0x3eb8aa3b, v46\n"
    "v_mul_f32 v70, 0x3eb8aa3b, v44\n"
    "s_waitcnt lgkmcnt(0)\n"
    "v_pk_mul_f32 v[48:49], v[48:49], v[60:61] op_sel_hi:[1,0]\n"
    "v_pk_mul_f32 v[50:51], v[50:51], v[60:61] op_sel_hi:[1,0]\n"
    "v_pk_fma_f32 v[48:49], v[36:37], v[62:63], v[48:49] op_sel_hi:[1,0,1]\n"
    "v_pk_fma_f32 v[50:51], v[38:39], v[62:63], v[50:51] op_sel_hi:[1,0,1]\n"
    "v_pk_mul_f32 v[52:53], v[52:53], v[68:69] op_sel_hi:[1,0]\n"
    "v_pk_mul_f32 v[54:55], v[54:55], v[68:69] op_sel_hi:[1,0]\n"
    "v_pk_fma_f32 v[52:53], v[36:37], v[70:71], v[52:53] op_sel_hi:[1,0,1]\n"
    "v_pk_fma_f32 v[54:55], v[38:39], v[70:71], v[54:55] op_sel_hi:[1,0,1]\n"
    "v_cvt_pk_bf16_f32 v28, v48, v49\n"
    "v_cvt_pk_bf16_f32 v29, v50, v51\n"
    "v_cvt_pk_bf16_f32 v30, v52, v53\n"
    "v_cvt_pk_bf16_f32 v31, v54, v55\n"
    "s_nop 0\n"
    "v_permlane16_swap_b32 v28, v30\n"
    "v_permlane16_swap_b32 v29, v31\n"
    "s_waitcnt vmcnt(3)\n"
    "ds_write_b128 v19, v[108:111]\n"
    "ds_write_b128 v20, v[112:115]\n"
    "ds_write_b128 v19, v[116:119] offset:16384\n"
    "ds_write_b128 v20, v[120:123] offset:16384\n"
    "ds_write_b128 v21, v[124:127] offset:32768\n"
    "v_cndmask_b32 v32, v32, v124, s[72:73]\n"
    "v_cndmask_b32 v33, v33, v125, s[72:73]\n"
    "v_cndmask_b32 v34, v34, v126, s[72:73]\n"
    "v_cndmask_b32 v35, v35, v127, s[72:73]\n"
    "v_mfma_f32_16x16x32_bf16 v[108:111], v[76:79], v[128:131], 0\n"
    "ds_read_b64_tr_b16 v[92:93], v16 offset:40960\n"
    "ds_read_b64_tr_b16 v[94:95], v16 offset:41472\n"
    "v_mfma_f32_16x16x32_bf16 v[112:115], v[76:79], v[132:135], 0\n"
    "ds_read_b64_tr_b16 v[96:97], v18 offset:40960\n"
    "ds_read_b64_tr_b16 v[98:99], v18 offset:41472\n"
    "v_mfma_f32_16x16x32_bf16 v[72:75], v[80:83], v[24:27], 0\n"
    "ds_read_b64_tr_b16 v[100:101], v16 offset:45056\n"
    "ds_read_b64_tr_b16 v[102:103], v16 offset:45568\n"
    "v_mfma_f32_16x16x32_bf16 v[108:111], v[84:87], v[136:139], v[108:111]\n"
    "ds_read_b64_tr_b16 v[104:105], v18 offset:45056\n"
    "ds_read_b64_tr_b16 v[106:107], v18 offset:45568\n"
    "v_mfma_f32_16x16x32_bf16 v[112:115], v[84:87], v[140:143], v[112:115]\n"
    "v_mfma_f32_16x16x32_bf16 v[72:75], v[88:91], v[24:27], v[72:75]\n"
    "s_waitcnt lgkmcnt(0)\n"
    "s_barrier\n"
    "v_mfma_f32_16x16x32_bf16 v[108:111], v[92:95], v[144:147], v[108:111]\n"
    "v_add_u32 v16, 0xffff0000, v16\n"
    "ds_read_b64_tr_b16 v[76:77], v16 offset:32768\n"
    "v_mfma_f32_16x16x32_bf16 v[112:115], v[92:95], v[148:151], v[112:115]\n"
    "ds_read_b64_tr_b16 v[78:79], v16 offset:33280\n"
    "v_add_u32 v18, 0xffff0000, v18\n"
    "v_mfma_f32_16x16x32_bf16 v[72:75], v[96:99], v[24:27], v[72:75]\n"
    "ds_read_b64_tr_b16 v[80:81], v18 offset:32768\n"
    "ds_read_b64_tr_b16 v[82:83], v18 offset:33280\n"
    "v_mfma_f32_16x16x32_bf16 v[108:111], v[100:103], v[152:155], v[108:111]\n"
    "ds_read_b64_tr_b16 v[84:85], v16 offset:36864\n"
    "ds_read_b64_tr_b16 v[86:87], v16 offset:37376\n"
    "v_mfma_f32_16x16x32_bf16 v[112:115], v[100:103], v[156:159], v[112:115]\n"
    "ds_read_b64_tr_b16 v[88:89], v18 offset:36864\n"
    "ds_read_b64_tr_b16 v[90:91], v18 offset:37376\n"
    "v_mfma_f32_16x16x32_bf16 v[72:75], v[104:107], v[24:27], v[72:75]\n"
    "s_waitcnt lgkmcnt(0)\n"
    "v_mfma_f32_16x16x32_bf16 v[108:111], v[76:79], v[160:163], v[108:111]\n"
    "ds_read_b64_tr_b16 v[92:93], v16 offset:40960\n"
    "ds_read_b64_tr_b16 v[94:95], v16 offset:41472\n"
    "v_mfma_f32_16x16x32_bf16 v[112:115], v[76:79], v[164:167], v[112:115]\n"
    "ds_read_b64_tr_b16 v[96:97], v18 offset:40960\n"
    "ds_read_b64_tr_b16 v[98:99], v18 offset:41472\n"
    "v_mfma_f32_16x16x32_bf16 v[72:75], v[80:83], v[24:27], v[72:75]\n"
    "ds_read_b64_tr_b16 v[100:101], v16 offset:45056\n"
    "ds_read_b64_tr_b16 v[102:103], v16 offset:45568\n"
    "v_mfma_f32_16x16x32_bf16 v[108:111], v[84:87], v[168:171], v[108:111]\n"
    "ds_read_b64_tr_b16 v[104:105], v18 offset:45056\n"
    "ds_read_b64_tr_b16 v[106:107], v18 offset:45568\n"
    "s_add_u32 m0, s61, 0x14000\n"
    "v_mfma_f32_16x16x32_bf16 v[112:115], v[84:87], v[172:175], v[112:115]\n"
    "s_add_u32 s71, s59, 0x20000\n"
    "buffer_load_dwordx4 v5, s[16:19], s71 offen lds\n"
    "s_add_u32 m0, s61, 0x16000\n"
    "v_mfma_f32_16x16x32_bf16 v[72:75], v[88:91], v[24:27], v[72:75]\n"
    "s_add_u32 s71, s59, 0x24000\n"
    "buffer_load_dwordx4 v5, s[16:19], s71 offen lds\n"
    "s_waitcnt lgkmcnt(0)\n"
    "s_barrier\n"
    "v_mfma_f32_16x16x32_bf16 v[108:111], v[92:95], v[176:179], v[108:111]\n"
    "ds_read_b64_tr_b16 v[76:77], v16\n"
    "ds_read_b64_tr_b16 v[78:79], v16 offset:512\n"
    "s_add_u32 m0, s61, 0x18000\n"
    "s_add_u32 s71, s59, 0x28000\n"
    "v_mfma_f32_16x16x32_bf16 v[112:115], v[92:95], v[180:183], v[112:115]\n"
    "ds_read_b64_tr_b16 v[80:81], v18\n"
    "ds_read_b64_tr_b16 v[82:83], v18 offset:512\n"
    "buffer_load_dwordx4 v5, s[16:19], s71 offen lds\n"
    "v_and_b32 v169, 31, v0\n"
    "v_mfma_f32_16x16x32_bf16 v[72:75], v[96:99], v[24:27], v[72:75]\n"
    "ds_read_b64_tr_b16 v[84:85], v16 offset:4096\n"
    "ds_read_b64_tr_b16 v[86:87], v16 offset:4608\n"
    "v_lshrrev_b32 v170, 5, v0\n"
    "v_lshlrev_b32 v171, 2, v170\n"
    "v_mfma_f32_16x16x32_bf16 v[108:111], v[100:103], v[184:187], v[108:111]\n"
    "ds_read_b64_tr_b16 v[88:89], v18 offset:4096\n"
    "ds_read_b64_tr_b16 v[90:91], v18 offset:4608\n"
    "v_sub_u32 v169, v169, v171\n"
    "v_cvt_f32_i32 v169, v169\n"
    "v_mfma_f32_16x16x32_bf16 v[112:115], v[100:103], v[188:191], v[112:115]\n"
    "v_mul_f32 v169, 0x4e800000, v169\n"
    "v_add_f32 v128, 0, v169\n"
    "v_med3_f32 v128, v128, s75, 0\n"
    "v_sub_f32 v144, s75, v128\n"
    "v_mfma_f32_16x16x32_bf16 v[72:75], v[104:107], v[24:27], v[72:75]\n"
    "v_add_f32 v129, 0xce800000, v169\n"
    "v_med3_f32 v129, v129, s75, 0\n"
    "v_sub_f32 v145, s75, v129\n"
    "s_waitcnt lgkmcnt(0)\n"
    "v_mfma_f32_16x16x32_bf16 v[108:111], v[76:79], v[192:195], v[108:111]\n"
    "ds_read_b64_tr_b16 v[92:93], v16 offset:8192\n"
    "ds_read_b64_tr_b16 v[94:95], v16 offset:8704\n"
    "s_waitcnt vmcnt(3)\n"
    "s_add_u32 s71, s49, 0x10000\n"
    "s_mov_b64 exec, 1\n"
    "buffer_store_dword v22, off, s[28:31], s71 sc1\n"
    "s_mov_b64 exec, -1\n"
    "s_add_u32 m0, s61, 0x1a000\n"
    "v_mfma_f32_16x16x32_bf16 v[112:115], v[76:79], v[196:199], v[112:115]\n"
    "ds_read_b64_tr_b16 v[96:97], v18 offset:8192\n"
    "ds_read_b64_tr_b16 v[98:99], v18 offset:8704\n"
    "s_add_u32 s71, s59, 0x2c000\n"
    "buffer_load_dwordx4 v5, s[16:19], s71 offen lds\n"
    "v_mfma_f32_16x16x32_bf16 v[72:75], v[80:83], v[24:27], v[72:75]\n"
    "ds_read_b64_tr_b16 v[100:101], v16 offset:12288\n"
    "ds_read_b64_tr_b16 v[102:103], v16 offset:12800\n"
    "s_add_u32 m0, s61, 0x8000\n"
    "s_add_u32 s71, s59, 0x40000\n"
    "v_mfma_f32_16x16x32_bf16 v[108:111], v[84:87], v[200:203], v[108:111]\n"
    "ds_read_b64_tr_b16 v[104:105], v18 offset:12288\n"
    "ds_read_b64_tr_b16 v[106:107], v18 offset:12800\n"
    "buffer_load_dwordx4 v5, s[16:19], s71 offen lds\n"
    "v_add_f32 v130, 0xcf000000, v169\n"
    "v_mfma_f32_16x16x32_bf16 v[112:115], v[84:87], v[204:207], v[112:115]\n"
    "v_med3_f32 v130, v130, s75, 0\n"
    "v_sub_f32 v146, s75, v130\n"
    "v_add_f32 v131, 0xcf400000, v169\n"
    "v_med3_f32 v131, v131, s75, 0\n"
    "v_mfma_f32_16x16x32_bf16 v[72:75], v[88:91], v[24:27], v[72:75]\n"
    "v_sub_f32 v147, s75, v131\n"
    "v_add_f32 v132, 0xd0000000, v169\n"
    "v_med3_f32 v132, v132, s75, 0\n"
    "s_waitcnt lgkmcnt(0)\n"
    "s_barrier\n"
    "v_mfma_f32_16x16x32_bf16 v[108:111], v[92:95], v[208:211], v[108:111]\n"
    "ds_read_b64_tr_b16 v[76:77], v16 offset:16384\n"
    "ds_read_b64_tr_b16 v[78:79], v16 offset:16896\n"
    "s_add_u32 m0, s61, 0xa000\n"
    "s_add_u32 s71, s59, 0x44000\n"
    "v_mfma_f32_16x16x32_bf16 v[112:115], v[92:95], v[212:215], v[112:115]\n"
    "ds_read_b64_tr_b16 v[80:81], v18 offset:16384\n"
    "ds_read_b64_tr_b16 v[82:83], v18 offset:16896\n"
    "buffer_load_dwordx4 v5, s[16:19], s71 offen lds\n"
    "s_mov_b32 m0, s61\n"
    "v_mfma_f32_16x16x32_bf16 v[72:75], v[96:99], v[24:27], v[72:75]\n"
    "ds_read_b64_tr_b16 v[84:85], v16 offset:20480\n"
    "ds_read_b64_tr_b16 v[86:87], v16 offset:20992\n"
    "s_add_u32 s71, s59, 0x48000\n"
    "buffer_load_dwordx4 v5, s[16:19], s71 offen lds\n"
    "v_mfma_f32_16x16x32_bf16 v[108:111], v[100:103], v[216:219], v[108:111]\n"
    "ds_read_b64_tr_b16 v[88:89], v18 offset:20480\n"
    "ds_read_b64_tr_b16 v[90:91], v18 offset:20992\n"
    "v_sub_f32 v148, s75, v132\n"
    "v_add_f32 v133, 0xd0100000, v169\n"
    "v_mfma_f32_16x16x32_bf16 v[112:115], v[100:103], v[220:223], v[112:115]\n"
    "v_med3_f32 v133, v133, s75, 0\n"
    "v_sub_f32 v149, s75, v133\n"
    "v_add_f32 v134, 0xd0200000, v169\n"
    "v_med3_f32 v134, v134, s75, 0\n"
    "v_mfma_f32_16x16x32_bf16 v[72:75], v[104:107], v[24:27], v[72:75]\n"
    "v_sub_f32 v150, s75, v134\n"
    "v_add_f32 v135, 0xd0300000, v169\n"
    "v_med3_f32 v135, v135, s75, 0\n"
    "s_waitcnt lgkmcnt(0)\n"
    "v_mfma_f32_16x16x32_bf16 v[108:111], v[76:79], v[224:227], v[108:111]\n"
    "ds_read_b64_tr_b16 v[92:93], v16 offset:24576\n"
    "ds_read_b64_tr_b16 v[94:95], v16 offset:25088\n"
    "s_add_u32 m0, s61, 0x2000\n"
    "s_add_u32 s71, s59, 0x4c000\n"
    "v_mfma_f32_16x16x32_bf16 v[112:115], v[76:79], v[228:231], v[112:115]\n"
    "ds_read_b64_tr_b16 v[96:97], v18 offset:24576\n"
    "ds_read_b64_tr_b16 v[98:99], v18 offset:25088\n"
    "buffer_load_dwordx4 v5, s[16:19], s71 offen lds\n"
    "v_sub_f32 v151, s75, v135\n"
    "v_mfma_f32_16x16x32_bf16 v[72:75], v[80:83], v[24:27], v[72:75]\n"
    "ds_read_b64_tr_b16 v[100:101], v16 offset:28672\n"
    "ds_read_b64_tr_b16 v[102:103], v16 offset:29184\n"
    "v_add_f32 v136, 0xd0800000, v169\n"
    "v_med3_f32 v136, v136, s75, 0\n"
    "v_mfma_f32_16x16x32_bf16 v[108:111], v[84:87], v[232:235], v[108:111]\n"
    "ds_read_b64_tr_b16 v[104:105], v18 offset:28672\n"
    "ds_read_b64_tr_b16 v[106:107], v18 offset:29184\n"
    "v_sub_f32 v152, s75, v136\n"
    "v_add_f32 v137, 0xd0880000, v169\n"
    "v_mfma_f32_16x16x32_bf16 v[112:115], v[84:87], v[236:239], v[112:115]\n"
    "v_med3_f32 v137, v137, s75, 0\n"
    "v_sub_f32 v153, s75, v137\n"
    "v_add_f32 v138, 0xd0900000, v169\n"
    "v_med3_f32 v138, v138, s75, 0\n"
    "v_mfma_f32_16x16x32_bf16 v[72:75], v[88:91], v[24:27], v[72:75]\n"
    "v_sub_f32 v154, s75, v138\n"
    "v_add_f32 v139, 0xd0980000, v169\n"
    "v_med3_f32 v139, v139, s75, 0\n"
    "s_waitcnt lgkmcnt(0)\n"
    "v_mfma_f32_16x16x32_bf16 v[108:111], v[92:95], v[240:243], v[108:111]\n"
    "v_sub_f32 v155, s75, v139\n"
    "v_add_f32 v140, 0xd0c00000, v169\n"
    "v_med3_f32 v140, v140, s75, 0\n"
    "v_sub_f32 v156, s75, v140\n"
    "v_mfma_f32_16x16x32_bf16 v[112:115], v[92:95], v[244:247], v[112:115]\n"
    "v_add_f32 v141, 0xd0c80000, v169\n"
    "v_med3_f32 v141, v141, s75, 0\n"
    "v_sub_f32 v157, s75, v141\n"
    "v_add_f32 v142, 0xd0d00000, v169\n"
    "v_mfma_f32_16x16x32_bf16 v[72:75], v[96:99], v[24:27], v[72:75]\n"
    "v_med3_f32 v142, v142, s75, 0\n"
    "v_sub_f32 v158, s75, v142\n"
    "v_add_f32 v143, 0xd0d80000, v169\n"
    "v_med3_f32 v143, v143, s75, 0\n"
    "v_mfma_f32_16x16x32_bf16 v[72:75], v[104:107], v[24:27], v[72:75]\n"
    "v_sub_f32 v159, s75, v143\n"
    "v_bfe_u32 v171, v0, 4, 1\n"
    "v_xor_b32 v169, v170, v171\n"
    "v_lshlrev_b32 v169, 7, v169\n"
    "v_mfma_f32_16x16x32_bf16 v[108:111], v[100:103], v[248:251], v[108:111]\n"
    "v_and_b32 v170, 15, v0\n"
    "v_lshl_add_u32 v169, v170, 3, v169\n"
    "s_lshl_b32 s66, s36, 12\n"
    "s_lshl_b32 s69, s37, 11\n"
    "v_mfma_f32_16x16x32_bf16 v[112:115], v[100:103], v[252:255], v[112:115]\n"
    "s_add_u32 s66, s66, s69\n"
    "s_add_u32 s66, s66, 0x24400\n"
    "s_sub_u32 s69, 0x27000, s66\n"
    "v_add_u32 v169, s66, v169\n"
    "v_mad_i32_i24 v160, v171, s69, v169\n"
    "v_add_u32 v170, -1, v171\n"
    "v_and_b32 v170, 0xfffff000, v170\n"
    "s_cmp_eq_u32 s65, 0\n"
    "s_cselect_b32 s66, 0xffff5000, 0\n"
    "v_add_u32 v161, v160, v170\n"
    "v_mad_i32_i24 v161, v171, s66, v161\n"
    "s_cmp_eq_u32 s65, 1\n"
    "s_cselect_b32 s66, 0xffff5000, 0\n"
    "v_add_u32 v162, v161, v170\n"
    "v_mad_i32_i24 v162, v171, s66, v162\n"
    "s_cmp_eq_u32 s65, 2\n"
    "s_cselect_b32 s66, 0xffff5000, 0\n"
    "v_add_u32 v163, v162, v170\n"
    "v_mad_i32_i24 v163, v171, s66, v163\n"
    "s_cmp_eq_u32 s65, 3\n"
    "s_cselect_b32 s66, 0xffff5000, 0\n"
    "v_add_u32 v164, v163, v170\n"
    "v_mad_i32_i24 v164, v171, s66, v164\n"
    "s_cmp_eq_u32 s65, 4\n"
    "s_cselect_b32 s66, 0xffff5000, 0\n"
    "v_add_u32 v165, v164, v170\n"
    "v_mad_i32_i24 v165, v171, s66, v165\n"
    "s_cmp_eq_u32 s65, 5\n"
    "s_cselect_b32 s66, 0xffff5000, 0\n"
    "v_add_u32 v166, v165, v170\n"
    "v_mad_i32_i24 v166, v171, s66, v166\n"
    "s_cmp_eq_u32 s65, 6\n"
    "s_cselect_b32 s66, 0xffff5000, 0\n"
    "v_add_u32 v167, v166, v170\n"
    "v_mad_i32_i24 v167, v171, s66, v167\n"
    "s_cmp_eq_u32 s65, 7\n"
    "s_cselect_b32 s66, 0xffff5000, 0\n"
    "v_add_u32 v168, v167, v170\n"
    "v_mad_i32_i24 v168, v171, s66, v168\n"
    "s_mov_b32 exec_lo, 0x10001\n"
    "s_mov_b32 exec_hi, 0x10001\n"
    "ds_write_b128 v11, v[72:75]\n"
    "s_mov_b64 exec, -1\n"
    "s_waitcnt vmcnt(7) lgkmcnt(0)\n"
    "s_barrier\n"
    "ds_read_b128 v[36:39], v13 offset:256\n"
    "s_add_u32 s71, s62, 0x10000\n"
    "buffer_load_dword v124, v2, s[28:31], s71 offen sc1\n"
    "v_add_u32 v23, 0x40000000, v1\n"
    "s_waitcnt lgkmcnt(0)\n"
    "v_pk_mul_f32 v[108:109], v[108:109], v[42:43] op_sel_hi:[1,0]\n"
    "v_pk_mul_f32 v[110:111], v[110:111], v[42:43] op_sel_hi:[1,0]\n"
    "v_pk_fma_f32 v[108:109], v[36:37], v[40:41], v[108:109] op_sel_hi:[1,0,1]\n"
    "v_pk_fma_f32 v[110:111], v[38:39], v[40:41], v[110:111] op_sel_hi:[1,0,1]\n"
    "v_pk_mul_f32 v[112:113], v[112:113], v[46:47] op_sel_hi:[1,0]\n"
    "v_pk_mul_f32 v[114:115], v[114:115], v[46:47] op_sel_hi:[1,0]\n"
    "v_pk_fma_f32 v[112:113], v[36:37], v[44:45], v[112:113] op_sel_hi:[1,0,1]\n"
    "v_pk_fma_f32 v[114:115], v[38:39], v[44:45], v[114:115] op_sel_hi:[1,0,1]\n"
    "v_cvt_pk_bf16_f32 v96, v108, v109\n"
    "v_cvt_pk_bf16_f32 v97, v110, v111\n"
    "v_cvt_pk_bf16_f32 v98, v112, v113\n"
    "v_cvt_pk_bf16_f32 v99, v114, v115\n"
    "v_pk_max_i16 v96, v96, 0\n"
    "v_pk_max_i16 v97, v97, 0\n"
    "v_pk_max_i16 v98, v98, 0\n"
    "v_pk_max_i16 v99, v99, 0\n"
    "s_nop 0\n"
    "v_permlane16_swap_b32 v96, v98\n"
    "v_permlane16_swap_b32 v97, v99\n"
    "buffer_store_dwordx4 v[96:99], v1, s[28:31], s51 offen sc1\n"
    "buffer_store_dwordx4 v[96:99], v23, s[28:31], s57 offen sc1\n"
    ".Lk_w1_merge:\n"
    "s_nop 1\n"
    "; ---- attention\n"
    "s_cmp_eq_u32 s37, 0\n"
    "s_cbranch_scc1 .Lk_np\n"
    "s_setprio 1\n"
    ".Lk_np:\n"
    "ds_read_b128 v[84:87], v19 offset:28672\n"
    "ds_read_b64_tr_b16 v[88:89], v160\n"
    "ds_read_b64_tr_b16 v[90:91], v160 offset:256\n"
    "ds_read_b64_tr_b16 v[92:93], v160 offset:512\n"
    "ds_read_b64_tr_b16 v[94:95], v160 offset:768\n"
    "v_mfma_f32_32x32x16_bf16 v[128:143], v[32:35], v[28:31], v[128:143]\n"
    "s_nop 11\n"
    "v_exp_f32 v128, v128\n"
    "v_exp_f32 v129, v129\n"
    "s_waitcnt lgkmcnt(0)\n"
    "v_exp_f32 v130, v130\n"
    "v_exp_f32 v131, v131\n"
    "v_cvt_pk_bf16_f32 v40, v128, v129\n"
    "ds_read_b64_tr_b16 v[236:237], v6 offset:49152\n"
    "ds_read_b64_tr_b16 v[238:239], v6 offset:49664\n"
    "v_exp_f32 v132, v132\n"
    "v_exp_f32 v133, v133\n"
    "v_cvt_pk_bf16_f32 v41, v130, v131\n"
    "ds_read_b64_tr_b16 v[240:241], v7 offset:49152\n"
    "ds_read_b64_tr_b16 v[242:243], v7 offset:49664\n"
    "v_exp_f32 v134, v134\n"
    "v_mfma_f32_32x32x16_bf16 v[48:63], v[84:87], v[28:31], 0\n"
    "ds_read_b128 v[80:83], v19 offset:24576\n"
    "v_exp_f32 v135, v135\n"
    "v_cvt_pk_bf16_f32 v42, v132, v133\n"
    "v_exp_f32 v136, v136\n"
    "v_cvt_pk_bf16_f32 v43, v134, v135\n"
    "ds_read_b64_tr_b16 v[244:245], v6 offset:57344\n"
    "v_exp_f32 v137, v137\n"
    "v_exp_f32 v138, v138\n"
    "ds_read_b64_tr_b16 v[246:247], v6 offset:57856\n"
    "ds_read_b64_tr_b16 v[248:249], v7 offset:57344\n"
    "v_exp_f32 v139, v139\n"
    "v_cvt_pk_bf16_f32 v44, v136, v137\n"
    "v_mfma_f32_32x32x16_bf16 v[64:79], v[88:91], v[40:43], 0\n"
    "ds_read_b64_tr_b16 v[88:89], v161\n"
    "ds_read_b64_tr_b16 v[90:91], v161 offset:256\n"
    "v_exp_f32 v140, v140\n"
    "v_exp_f32 v141, v141\n"
    "v_cvt_pk_bf16_f32 v45, v138, v139\n"
    "ds_read_b64_tr_b16 v[250:251], v7 offset:57856\n"
    "s_add_u32 m0, s61, 0x4000\n"
    "s_add_u32 s71, s59, 0x60000\n"
    "buffer_load_dwordx4 v5, s[16:19], s71 offen lds\n"
    "v_exp_f32 v142, v142\n"
    "v_exp_f32 v143, v143\n"
    "v_cvt_pk_bf16_f32 v46, v140, v141\n"
    "s_add_u32 m0, s61, 0x6000\n"
    "s_add_u32 s71, s59, 0x64000\n"
    "buffer_load_dwordx4 v5, s[16:19], s71 offen lds\n"
    "v_cvt_pk_bf16_f32 v47, v142, v143\n"
    "v_exp_f32 v48, v48\n"
    "v_exp_f32 v49, v49\n"
    "s_waitcnt lgkmcnt(0)\n"
    "v_mfma_f32_32x32x16_bf16 v[64:79], v[92:95], v[44:47], v[64:79]\n"
    "ds_read_b64_tr_b16 v[92:93], v161 offset:512\n"
    "ds_read_b64_tr_b16 v[94:95], v161 offset:768\n"
    "v_exp_f32 v50, v50\n"
    "v_exp_f32 v51, v51\n"
    "v_cvt_pk_bf16_f32 v40, v48, v49\n"
    "v_add_u32 v6, 0x10000, v6\n"
    "ds_read_b64_tr_b16 v[252:253], v6\n"
    "ds_read_b64_tr_b16 v[254:255], v6 offset:512\n"
    "v_exp_f32 v52, v52\n"
    "v_exp_f32 v53, v53\n"
    "v_cvt_pk_bf16_f32 v41, v50, v51\n"
    "v_add_u32 v7, 0x10000, v7\n"
    "ds_read_b64_tr_b16 v[96:97], v7\n"
    "v_exp_f32 v54, v54\n"
    "v_mfma_f32_32x32x16_bf16 v[128:143], v[80:83], v[28:31], 0\n"
    "ds_read_b128 v[84:87], v19 offset:20480\n"
    "v_exp_f32 v55, v55\n"
    "v_cvt_pk_bf16_f32 v42, v52, v53\n"
    "v_exp_f32 v56, v56\n"
    "v_cvt_pk_bf16_f32 v43, v54, v55\n"
    "ds_read_b64_tr_b16 v[98:99], v7 offset:512\n"
    "ds_read_b64_tr_b16 v[100:101], v6 offset:8192\n"
    "v_exp_f32 v57, v57\n"
    "v_exp_f32 v58, v58\n"
    "ds_read_b64_tr_b16 v[102:103], v6 offset:8704\n"
    "ds_read_b64_tr_b16 v[104:105], v7 offset:8192\n"
    "v_exp_f32 v59, v59\n"
    "v_cvt_pk_bf16_f32 v44, v56, v57\n"
    "v_mfma_f32_32x32x16_bf16 v[64:79], v[88:91], v[40:43], v[64:79]\n"
    "ds_read_b64_tr_b16 v[88:89], v162\n"
    "ds_read_b64_tr_b16 v[90:91], v162 offset:256\n"
    "v_exp_f32 v60, v60\n"
    "v_exp_f32 v61, v61\n"
    "v_cvt_pk_bf16_f32 v45, v58, v59\n"
    "ds_read_b64_tr_b16 v[106:107], v7 offset:8704\n"
    "s_waitcnt vmcnt(4)\n"
    "v_cmp_ne_u32 vcc, v22, v124\n"
    "s_cbranch_vccnz .Lk_spin_7\n"
    ".Lk_ok_8:\n"
    "v_exp_f32 v62, v62\n"
    "v_exp_f32 v63, v63\n"
    "v_cvt_pk_bf16_f32 v46, v60, v61\n"
    "s_mov_b32 s68, s58\n"
    "buffer_load_dwordx4 v[172:175], v1, s[28:31], s68 offen sc1\n"
    "buffer_load_dwordx4 v[176:179], v1, s[28:31], s68 offen offset:1024 sc1\n"
    "v_cvt_pk_bf16_f32 v47, v62, v63\n"
    "v_exp_f32 v128, v128\n"
    "v_exp_f32 v129, v129\n"
    "s_waitcnt lgkmcnt(0)\n"
    "v_mfma_f32_32x32x16_bf16 v[64:79], v[92:95], v[44:47], v[64:79]\n"
    "ds_read_b64_tr_b16 v[92:93], v162 offset:512\n"
    "ds_read_b64_tr_b16 v[94:95], v162 offset:768\n"
    "v_exp_f32 v130, v130\n"
    "v_exp_f32 v131, v131\n"
    "v_cvt_pk_bf16_f32 v40, v128, v129\n"
    "s_waitcnt vmcnt(4)\n"
    "s_add_u32 s71, s49, 0x18000\n"
    "s_mov_b64 exec, 1\n"
    "buffer_store_dword v22, off, s[28:31], s71 sc1\n"
    "s_mov_b64 exec, -1\n"
    "ds_read_b64_tr_b16 v[108:109], v6 offset:16384\n"
    "v_exp_f32 v132, v132\n"
    "v_exp_f32 v133, v133\n"
    "v_cvt_pk_bf16_f32 v41, v130, v131\n"
    "ds_read_b64_tr_b16 v[110:111], v6 offset:16896\n"
    "ds_read_b64_tr_b16 v[112:113], v7 offset:16384\n"
    "v_exp_f32 v134, v134\n"
    "v_mfma_f32_32x32x16_bf16 v[48:63], v[84:87], v[28:31], 0\n"
    "ds_read_b128 v[80:83], v19 offset:16384\n"
    "v_exp_f32 v135, v135\n"
    "v_cvt_pk_bf16_f32 v42, v132, v133\n"
    "v_exp_f32 v136, v136\n"
    "v_cvt_pk_bf16_f32 v43, v134, v135\n"
    "ds_read_b64_tr_b16 v[114:115], v7 offset:16896\n"
    "ds_read_b64_tr_b16 v[116:117], v6 offset:24576\n"
    "v_exp_f32 v137, v137\n"
    "v_exp_f32 v138, v138\n"
    "ds_read_b64_tr_b16 v[118:119], v6 offset:25088\n"
    "ds_read_b64_tr_b16 v[120:121], v7 offset:24576\n"
    "v_exp_f32 v139, v139\n"
    "v_cvt_pk_bf16_f32 v44, v136, v137\n"
    "v_mfma_f32_32x32x16_bf16 v[64:79], v[88:91], v[40:43], v[64:79]\n"
    "ds_read_b64_tr_b16 v[88:89], v163\n"
    "ds_read_b64_tr_b16 v[90:91], v163 offset:256\n"
    "v_exp_f32 v140, v140\n"
    "v_exp_f32 v141, v141\n"
    "v_cvt_pk_bf16_f32 v45, v138, v139\n"
    "ds_read_b64_tr_b16 v[122:123], v7 offset:25088\n"
    "buffer_load_dwordx4 v[180:183], v1, s[28:31], s68 offen offset:2048 sc1\n"
    "v_exp_f32 v142, v142\n"
    "v_exp_f32 v143, v143\n"
    "v_cvt_pk_bf16_f32 v46, v140, v141\n"
    "buffer_load_dwordx4 v[184:187], v1, s[28:31], s68 offen offset:3072 sc1\n"
    "v_cvt_pk_bf16_f32 v47, v142, v143\n"
    "v_exp_f32 v48, v48\n"
    "v_exp_f32 v49, v49\n"
    "s_waitcnt lgkmcnt(0)\n"
    "s_barrier\n"
    "v_mfma_f32_32x32x16_bf16 v[64:79], v[92:95], v[44:47], v[64:79]\n"
    "ds_read_b64_tr_b16 v[92:93], v163 offset:512\n"
    "ds_read_b64_tr_b16 v[94:95], v163 offset:768\n"
    "v_exp_f32 v50, v50\n"
    "v_exp_f32 v51, v51\n"
    "v_cvt_pk_bf16_f32 v40, v48, v49\n"
    "s_add_u32 m0, s61, 0xc000\n"
    "s_add_u32 s71, s59, 0x68000\n"
    "buffer_load_dwordx4 v5, s[16:19], s71 offen lds\n"
    "v_exp_f32 v52, v52\n"
    "v_exp_f32 v53, v53\n"
    "v_cvt_pk_bf16_f32 v41, v50, v51\n"
    "v_exp_f32 v54, v54\n"
    "v_mfma_f32_32x32x16_bf16 v[128:143], v[80:83], v[28:31], 0\n"
    "ds_read_b128 v[84:87], v19 offset:12288\n"
    "v_exp_f32 v55, v55\n"
    "v_cvt_pk_bf16_f32 v42, v52, v53\n"
    "v_exp_f32 v56, v56\n"
    "v_cvt_pk_bf16_f32 v43, v54, v55\n"
    "s_add_u32 s68, s58, 0x2000\n"
    "buffer_load_dwordx4 v[188:191], v1, s[28:31], s68 offen sc1\n"
    "v_exp_f32 v57, v57\n"
    "v_exp_f32 v58, v58\n"
    "v_exp_f32 v59, v59\n"
    "v_cvt_pk_bf16_f32 v44, v56, v57\n"
    "v_mfma_f32_32x32x16_bf16 v[64:79], v[88:91], v[40:43], v[64:79]\n"
    "ds_read_b64_tr_b16 v[88:89], v164\n"
    "ds_read_b64_tr_b16 v[90:91], v164 offset:256\n"
    "v_exp_f32 v60, v60\n"
    "v_exp_f32 v61, v61\n"
    "v_cvt_pk_bf16_f32 v45, v58, v59\n"
    "buffer_load_dwordx4 v[192:195], v1, s[28:31], s68 offen offset:1024 sc1\n"
    "v_exp_f32 v62, v62\n"
    "v_exp_f32 v63, v63\n"
    "v_cvt_pk_bf16_f32 v46, v60, v61\n"
    "v_cvt_pk_bf16_f32 v47, v62, v63\n"
    "v_exp_f32 v128, v128\n"
    "v_exp_f32 v129, v129\n"
    "s_waitcnt lgkmcnt(0)\n"
    "v_mfma_f32_32x32x16_bf16 v[64:79], v[92:95], v[44:47], v[64:79]\n"
    "ds_read_b64_tr_b16 v[92:93], v164 offset:512\n"
    "ds_read_b64_tr_b16 v[94:95], v164 offset:768\n"
    "v_exp_f32 v130, v130\n"
    "v_exp_f32 v131, v131\n"
    "v_cvt_pk_bf16_f32 v40, v128, v129\n"
    "s_add_u32 m0, s61, 0xe000\n"
    "s_add_u32 s71, s59, 0x6c000\n"
    "buffer_load_dwordx4 v5, s[16:19], s71 offen lds\n"
    "v_exp_f32 v132, v132\n"
    "v_exp_f32 v133, v133\n"
    "v_cvt_pk_bf16_f32 v41, v130, v131\n"
    "v_exp_f32 v134, v134\n"
    "v_mfma_f32_32x32x16_bf16 v[48:63], v[84:87], v[28:31], 0\n"
    "ds_read_b128 v[80:83], v19 offset:8192\n"
    "v_exp_f32 v135, v135\n"
    "v_cvt_pk_bf16_f32 v42, v132, v133\n"
    "v_exp_f32 v136, v136\n"
    "v_cvt_pk_bf16_f32 v43, v134, v135\n"
    "buffer_load_dwordx4 v[196:199], v1, s[28:31], s68 offen offset:2048 sc1\n"
    "v_exp_f32 v137, v137\n"
    "v_exp_f32 v138, v138\n"
    "v_exp_f32 v139, v139\n"
    "v_cvt_pk_bf16_f32 v44, v136, v137\n"
    "v_mfma_f32_32x32x16_bf16 v[64:79], v[88:91], v[40:43], v[64:79]\n"
    "ds_read_b64_tr_b16 v[88:89], v165\n"
    "ds_read_b64_tr_b16 v[90:91], v165 offset:256\n"
    "v_exp_f32 v140, v140\n"
    "v_exp_f32 v141, v141\n"
    "v_cvt_pk_bf16_f32 v45, v138, v139\n"
    "buffer_load_dwordx4 v[200:203], v1, s[28:31], s68 offen offset:3072 sc1\n"
    "v_exp_f32 v142, v142\n"
    "v_exp_f32 v143, v143\n"
    "v_cvt_pk_bf16_f32 v46, v140, v141\n"
    "v_cvt_pk_bf16_f32 v47, v142, v143\n"
    "s_setprio 0\n"
    "v_exp_f32 v48, v48\n"
    "v_exp_f32 v49, v49\n"
    "s_waitcnt lgkmcnt(0)\n"
    "v_mfma_f32_32x32x16_bf16 v[64:79], v[92:95], v[44:47], v[64:79]\n"
    "ds_read_b64_tr_b16 v[92:93], v165 offset:512\n"
    "ds_read_b64_tr_b16 v[94:95], v165 offset:768\n"
    "v_exp_f32 v50, v50\n"
    "v_exp_f32 v51, v51\n"
    "v_cvt_pk_bf16_f32 v40, v48, v49\n"
    "s_add_u32 m0, s61, 0x10000\n"
    "s_add_u32 s71, s59, 0x10000\n"
    "buffer_load_dwordx4 v5, s[16:19], s71 offen lds\n"
    "v_exp_f32 v52, v52\n"
    "v_exp_f32 v53, v53\n"
    "v_cvt_pk_bf16_f32 v41, v50, v51\n"
    "v_exp_f32 v54, v54\n"
    "v_mfma_f32_32x32x16_bf16 v[128:143], v[80:83], v[28:31], 0\n"
    "ds_read_b128 v[84:87], v19 offset:4096\n"
    "v_exp_f32 v55, v55\n"
    "v_cvt_pk_bf16_f32 v42, v52, v53\n"
    "v_exp_f32 v56, v56\n"
    "v_cvt_pk_bf16_f32 v43, v54, v55\n"
    "s_add_u32 s68, s58, 0x4000\n"
    "buffer_load_dwordx4 v[204:207], v1, s[28:31], s68 offen sc1\n"
    "v_exp_f32 v57, v57\n"
    "v_exp_f32 v58, v58\n"
    "v_exp_f32 v59, v59\n"
    "v_cvt_pk_bf16_f32 v44, v56, v57\n"
    "v_mfma_f32_32x32x16_bf16 v[64:79], v[88:91], v[40:43], v[64:79]\n"
    "ds_read_b64_tr_b16 v[88:89], v166\n"
    "ds_read_b64_tr_b16 v[90:91], v166 offset:256\n"
    "v_exp_f32 v60, v60\n"
    "v_exp_f32 v61, v61\n"
    "v_cvt_pk_bf16_f32 v45, v58, v59\n"
    "buffer_load_dwordx4 v[208:211], v1, s[28:31], s68 offen offset:1024 sc1\n"
    "v_exp_f32 v62, v62\n"
    "v_exp_f32 v63, v63\n"
    "v_cvt_pk_bf16_f32 v46, v60, v61\n"
    "v_cvt_pk_bf16_f32 v47, v62, v63\n"
    "v_exp_f32 v128, v128\n"
    "v_exp_f32 v129, v129\n"
    "s_waitcnt lgkmcnt(0)\n"
    "v_mfma_f32_32x32x16_bf16 v[64:79], v[92:95], v[44:47], v[64:79]\n"
    "ds_read_b64_tr_b16 v[92:93], v166 offset:512\n"
    "ds_read_b64_tr_b16 v[94:95], v166 offset:768\n"
    "v_exp_f32 v130, v130\n"
    "v_exp_f32 v131, v131\n"
    "v_cvt_pk_bf16_f32 v40, v128, v129\n"
    "s_add_u32 m0, s61, 0x12000\n"
    "s_add_u32 s71, s59, 0x14000\n"
    "buffer_load_dwordx4 v5, s[16:19], s71 offen lds\n"
    "v_exp_f32 v132, v132\n"
    "v_exp_f32 v133, v133\n"
    "v_cvt_pk_bf16_f32 v41, v130, v131\n"
    "v_exp_f32 v134, v134\n"
    "v_mfma_f32_32x32x16_bf16 v[48:63], v[84:87], v[28:31], 0\n"
    "ds_read_b128 v[80:83], v19\n"
    "v_exp_f32 v135, v135\n"
    "v_cvt_pk_bf16_f32 v42, v132, v133\n"
    "v_exp_f32 v136, v136\n"
    "v_cvt_pk_bf16_f32 v43, v134, v135\n"
    "buffer_load_dwordx4 v[212:215], v1, s[28:31], s68 offen offset:2048 sc1\n"
    "v_exp_f32 v137, v137\n"
    "v_exp_f32 v138, v138\n"
    "v_exp_f32 v139, v139\n"
    "v_cvt_pk_bf16_f32 v44, v136, v137\n"
    "v_mfma_f32_32x32x16_bf16 v[64:79], v[88:91], v[40:43], v[64:79]\n"
    "ds_read_b64_tr_b16 v[88:89], v167\n"
    "ds_read_b64_tr_b16 v[90:91], v167 offset:256\n"
    "v_exp_f32 v140, v140\n"
    "v_exp_f32 v141, v141\n"
    "v_cvt_pk_bf16_f32 v45, v138, v139\n"
    "buffer_load_dwordx4 v[216:219], v1, s[28:31], s68 offen offset:3072 sc1\n"
    "v_exp_f32 v142, v142\n"
    "v_exp_f32 v143, v143\n"
    "v_cvt_pk_bf16_f32 v46, v140, v141\n"
    "v_cvt_pk_bf16_f32 v47, v142, v143\n"
    "v_exp_f32 v48, v48\n"
    "v_exp_f32 v49, v49\n"
    "s_waitcnt lgkmcnt(0)\n"
    "v_mfma_f32_32x32x16_bf16 v[64:79], v[92:95], v[44:47], v[64:79]\n"
    "ds_read_b64_tr_b16 v[92:93], v167 offset:512\n"
    "ds_read_b64_tr_b16 v[94:95], v167 offset:768\n"
    "v_exp_f32 v50, v50\n"
    "v_exp_f32 v51, v51\n"
    "v_cvt_pk_bf16_f32 v40, v48, v49\n"
    "s_add_u32 m0, s61, 0x14000\n"
    "s_add_u32 s71, s59, 0x18000\n"
    "buffer_load_dwordx4 v5, s[16:19], s71 offen lds\n"
    "v_exp_f32 v52, v52\n"
    "v_exp_f32 v53, v53\n"
    "v_cvt_pk_bf16_f32 v41, v50, v51\n"
    "v_exp_f32 v54, v54\n"
    "v_mfma_f32_32x32x16_bf16 v[128:143], v[80:83], v[28:31], v[144:159]\n"
    "v_exp_f32 v55, v55\n"
    "v_cvt_pk_bf16_f32 v42, v52, v53\n"
    "v_exp_f32 v56, v56\n"
    "v_cvt_pk_bf16_f32 v43, v54, v55\n"
    "s_add_u32 s68, s58, 0x6000\n"
    "buffer_load_dwordx4 v[220:223], v1, s[28:31], s68 offen sc1\n"
    "v_exp_f32 v57, v57\n"
    "v_exp_f32 v58, v58\n"
    "v_exp_f32 v59, v59\n"
    "v_cvt_pk_bf16_f32 v44, v56, v57\n"
    "v_mfma_f32_32x32x16_bf16 v[64:79], v[88:91], v[40:43], v[64:79]\n"
    "ds_read_b64_tr_b16 v[88:89], v168\n"
    "ds_read_b64_tr_b16 v[90:91], v168 offset:256\n"
    "v_exp_f32 v60, v60\n"
    "v_exp_f32 v61, v61\n"
    "v_cvt_pk_bf16_f32 v45, v58, v59\n"
    "buffer_load_dwordx4 v[224:227], v1, s[28:31], s68 offen offset:1024 sc1\n"
    "v_exp_f32 v62, v62\n"
    "v_exp_f32 v63, v63\n"
    "v_cvt_pk_bf16_f32 v46, v60, v61\n"
    "v_cvt_pk_bf16_f32 v47, v62, v63\n"
    "v_exp_f32 v128, v128\n"
    "v_exp_f32 v129, v129\n"
    "s_waitcnt lgkmcnt(0)\n"
    "v_mfma_f32_32x32x16_bf16 v[64:79], v[92:95], v[44:47], v[64:79]\n"
    "ds_read_b64_tr_b16 v[92:93], v168 offset:512\n"
    "ds_read_b64_tr_b16 v[94:95], v168 offset:768\n"
    "v_exp_f32 v130, v130\n"
    "v_exp_f32 v131, v131\n"
    "v_cvt_pk_bf16_f32 v40, v128, v129\n"
    "s_add_u32 s71, s62, 0x18000\n"
    "buffer_load_dword v124, v2, s[28:31], s71 offen sc1\n"
    "v_exp_f32 v132, v132\n"
    "v_exp_f32 v133, v133\n"
    "v_cvt_pk_bf16_f32 v41, v130, v131\n"
    "s_add_u32 m0, s61, 0x16000\n"
    "s_add_u32 s71, s59, 0x1c000\n"
    "buffer_load_dwordx4 v5, s[16:19], s71 offen lds\n"
    "v_exp_f32 v134, v134\n"
    "v_exp_f32 v135, v135\n"
    "v_cvt_pk_bf16_f32 v42, v132, v133\n"
    "v_exp_f32 v136, v136\n"
    "v_cvt_pk_bf16_f32 v43, v134, v135\n"
    "v_exp_f32 v137, v137\n"
    "v_exp_f32 v138, v138\n"
    "buffer_load_dwordx4 v[228:231], v1, s[28:31], s68 offen offset:2048 sc1\n"
    "v_exp_f32 v139, v139\n"
    "v_cvt_pk_bf16_f32 v44, v136, v137\n"
    "v_mfma_f32_32x32x16_bf16 v[64:79], v[88:91], v[40:43], v[64:79]\n"
    "v_exp_f32 v140, v140\n"
    "v_exp_f32 v141, v141\n"
    "v_cvt_pk_bf16_f32 v45, v138, v139\n"
    "buffer_load_dwordx4 v[232:235], v1, s[28:31], s68 offen offset:3072 sc1\n"
    "v_exp_f32 v142, v142\n"
    "v_exp_f32 v143, v143\n"
    "v_cvt_pk_bf16_f32 v46, v140, v141\n"
    "v_cvt_pk_bf16_f32 v47, v142, v143\n"
    "s_waitcnt lgkmcnt(0)\n"
    "s_nop 0\n"
    "v_mfma_f32_32x32x16_bf16 v[64:79], v[92:95], v[44:47], v[64:79]\n"
    "s_nop 11\n"
    "v_add_f32 v128, abs(v64), abs(v65)\n"
    "v_add_f32 v129, abs(v66), abs(v67)\n"
    "v_add_f32 v128, abs(v68), v128\n"
    "v_add_f32 v129, abs(v69), v129\n"
    "v_add_f32 v128, abs(v70), v128\n"
    "v_add_f32 v129, abs(v71), v129\n"
    "v_add_f32 v128, v128, v129\n"
    "v_cmp_gt_f32 vcc, 0x7b800000, v128\n"
    "s_mov_b64 s[72:73], vcc\n"
    "v_cmp_lt_f32 vcc, 0x21800000, v72\n"
    "s_and_b64 s[72:73], s[72:73], vcc\n"
    "v_cmp_gt_f32 vcc, 0x5d800000, v72\n"
    "s_and_b64 s[72:73], s[72:73], vcc\n"
    "s_cmp_eq_u64 s[72:73], -1\n"
    "s_cbranch_scc0 .Lk_slow\n"
    ".Lk_norm:\n"
    "v_rcp_f32 v132, v72\n"
    "s_nop 0\n"
    "v_pk_mul_f32 v[64:65], v[64:65], v[132:133] op_sel_hi:[1,0]\n"
    "v_pk_mul_f32 v[66:67], v[66:67], v[132:133] op_sel_hi:[1,0]\n"
    "v_pk_mul_f32 v[68:69], v[68:69], v[132:133] op_sel_hi:[1,0]\n"
    "v_pk_mul_f32 v[70:71], v[70:71], v[132:133] op_sel_hi:[1,0]\n"
    "v_cvt_pk_bf16_f32 v136, v64, v65\n"
    "v_cvt_pk_bf16_f32 v137, v66, v67\n"
    "v_cvt_pk_bf16_f32 v138, v68, v69\n"
    "v_cvt_pk_bf16_f32 v139, v70, v71\n"
    "s_nop 0\n"
    "v_permlane32_swap_b32 v136, v138\n"
    "v_permlane32_swap_b32 v137, v139\n"
    "buffer_store_dwordx4 v[136:139], v1, s[28:31], s64 offen sc1\n"
    "; ---- P3\n"
    "s_waitcnt vmcnt(25) lgkmcnt(0)\n"
    "s_barrier\n"
    "s_waitcnt vmcnt(24)\n"
    "v_mfma_f32_32x32x16_bf16 v[48:63], v[172:175], v[236:239], 0\n"
    "s_add_u32 m0, s61, 0x1c000\n"
    "s_add_u32 s71, s59, 0x30000\n"
    "buffer_load_dwordx4 v5, s[16:19], s71 offen lds\n"
    "s_add_u32 m0, s61, 0x1e000\n"
    "s_add_u32 s71, s59, 0x34000\n"
    "v_mfma_f32_32x32x16_bf16 v[64:79], v[172:175], v[240:243], 0\n"
    "buffer_load_dwordx4 v5, s[16:19], s71 offen lds\n"
    "s_waitcnt vmcnt(25)\n"
    "v_mfma_f32_32x32x16_bf16 v[48:63], v[176:179], v[244:247], v[48:63]\n"
    "s_add_u32 m0, s61, 0x20000\n"
    "s_add_u32 s71, s59, 0x38000\n"
    "buffer_load_dwordx4 v5, s[16:19], s71 offen lds\n"
    "v_mfma_f32_32x32x16_bf16 v[64:79], v[176:179], v[248:251], v[64:79]\n"
    "s_waitcnt vmcnt(24)\n"
    "v_mfma_f32_32x32x16_bf16 v[48:63], v[180:183], v[252:255], v[48:63]\n"
    "s_waitcnt vmcnt(7)\n"
    "v_cmp_ne_u32 vcc, v22, v124\n"
    "s_cbranch_vccnz .Lk_spin_9\n"
    ".Lk_ok_10:\n"
    "s_add_u32 s68, s58, 0x1000\n"
    "buffer_load_dwordx4 v[236:239], v1, s[28:31], s68 offen sc1\n"
    "v_mfma_f32_32x32x16_bf16 v[64:79], v[180:183], v[96:99], v[64:79]\n"
    "v_mfma_f32_32x32x16_bf16 v[48:63], v[184:187], v[100:103], v[48:63]\n"
    "buffer_load_dwordx4 v[240:243], v1, s[28:31], s68 offen offset:1024 sc1\n"
    "buffer_load_dwordx4 v[244:247], v1, s[28:31], s68 offen offset:2048 sc1\n"
    "v_mfma_f32_32x32x16_bf16 v[64:79], v[184:187], v[104:107], v[64:79]\n"
    "v_mfma_f32_32x32x16_bf16 v[48:63], v[188:191], v[108:111], v[48:63]\n"
    "buffer_load_dwordx4 v[248:251], v1, s[28:31], s68 offen offset:3072 sc1\n"
    "s_add_u32 s68, s58, 0x3000\n"
    "buffer_load_dwordx4 v[252:255], v1, s[28:31], s68 offen sc1\n"
    "v_mfma_f32_32x32x16_bf16 v[64:79], v[188:191], v[112:115], v[64:79]\n"
    "v_mfma_f32_32x32x16_bf16 v[48:63], v[192:195], v[116:119], v[48:63]\n"
    "ds_read_b64_tr_b16 v[80:81], v6 offset:32768\n"
    "ds_read_b64_tr_b16 v[82:83], v6 offset:33280\n"
    "ds_read_b64_tr_b16 v[84:85], v7 offset:32768\n"
    "ds_read_b64_tr_b16 v[86:87], v7 offset:33280\n"
    "s_waitcnt vmcnt(8)\n"
    "s_add_u32 s71, s49, 0x20000\n"
    "s_mov_b64 exec, 1\n"
    "buffer_store_dword v22, off, s[28:31], s71 sc1\n"
    "s_mov_b64 exec, -1\n"
    "v_mfma_f32_32x32x16_bf16 v[64:79], v[192:195], v[120:123], v[64:79]\n"
    "buffer_load_dwordx4 v[96:99], v1, s[28:31], s68 offen offset:1024 sc1\n"
    "buffer_load_dwordx4 v[100:103], v1, s[28:31], s68 offen offset:2048 sc1\n"
    "s_waitcnt lgkmcnt(0)\n"
    "v_mfma_f32_32x32x16_bf16 v[48:63], v[196:199], v[80:83], v[48:63]\n"
    "ds_read_b64_tr_b16 v[88:89], v6 offset:40960\n"
    "ds_read_b64_tr_b16 v[90:91], v6 offset:41472\n"
    "ds_read_b64_tr_b16 v[92:93], v7 offset:40960\n"
    "ds_read_b64_tr_b16 v[94:95], v7 offset:41472\n"
    "buffer_load_dwordx4 v[104:107], v1, s[28:31], s68 offen offset:3072 sc1\n"
    "v_mfma_f32_32x32x16_bf16 v[64:79], v[196:199], v[84:87], v[64:79]\n"
    "s_add_u32 s68, s58, 0x5000\n"
    "buffer_load_dwordx4 v[108:111], v1, s[28:31], s68 offen sc1\n"
    "s_waitcnt lgkmcnt(0)\n"
    "s_barrier\n"
    "v_mfma_f32_32x32x16_bf16 v[48:63], v[200:203], v[88:91], v[48:63]\n"
    "v_add_u32 v6, 0xffff0000, v6\n"
    "ds_read_b64_tr_b16 v[80:81], v6 offset:32768\n"
    "ds_read_b64_tr_b16 v[82:83], v6 offset:33280\n"
    "v_add_u32 v7, 0xffff0000, v7\n"
    "buffer_load_dwordx4 v[112:115], v1, s[28:31], s68 offen offset:1024 sc1\n"
    "buffer_load_dwordx4 v[116:119], v1, s[28:31], s68 offen offset:2048 sc1\n"
    "v_mfma_f32_32x32x16_bf16 v[64:79], v[200:203], v[92:95], v[64:79]\n"
    "ds_read_b64_tr_b16 v[84:85], v7 offset:32768\n"
    "ds_read_b64_tr_b16 v[86:87], v7 offset:33280\n"
    "s_add_u32 m0, s61, 0x18000\n"
    "s_add_u32 s71, s59, 0x58000\n"
    "buffer_load_dwordx4 v5, s[16:19], s71 offen lds\n"
    "s_waitcnt lgkmcnt(0)\n"
    "v_mfma_f32_32x32x16_bf16 v[48:63], v[204:207], v[80:83], v[48:63]\n"
    "ds_read_b64_tr_b16 v[88:89], v6 offset:40960\n"
    "ds_read_b64_tr_b16 v[90:91], v6 offset:41472\n"
    "ds_read_b64_tr_b16 v[92:93], v7 offset:40960\n"
    "ds_read_b64_tr_b16 v[94:95], v7 offset:41472\n"
    "buffer_load_dwordx4 v[120:123], v1, s[28:31], s68 offen offset:3072 sc1\n"
    "v_mfma_f32_32x32x16_bf16 v[64:79], v[204:207], v[84:87], v[64:79]\n"
    "s_add_u32 m0, s61, 0x1a000\n"
    "s_add_u32 s71, s59, 0x5c000\n"
    "buffer_load_dwordx4 v5, s[16:19], s71 offen lds\n"
    "s_waitcnt lgkmcnt(0)\n"
    "v_mfma_f32_32x32x16_bf16 v[48:63], v[208:211], v[88:91], v[48:63]\n"
    "ds_read_b64_tr_b16 v[80:81], v6\n"
    "ds_read_b64_tr_b16 v[82:83], v6 offset:512\n"
    "ds_read_b64_tr_b16 v[84:85], v7\n"
    "ds_read_b64_tr_b16 v[86:87], v7 offset:512\n"
    "s_add_u32 m0, s61, 0x22000\n"
    "v_mfma_f32_32x32x16_bf16 v[64:79], v[208:211], v[92:95], v[64:79]\n"
    "s_add_u32 s71, s59, 0x3c000\n"
    "buffer_load_dwordx4 v5, s[16:19], s71 offen lds\n"
    "s_waitcnt lgkmcnt(0)\n"
    "v_mfma_f32_32x32x16_bf16 v[48:63], v[212:215], v[80:83], v[48:63]\n"
    "ds_read_b64_tr_b16 v[88:89], v6 offset:8192\n"
    "ds_read_b64_tr_b16 v[90:91], v6 offset:8704\n"
    "ds_read_b64_tr_b16 v[92:93], v7 offset:8192\n"
    "ds_read_b64_tr_b16 v[94:95], v7 offset:8704\n"
    "s_add_u32 m0, s61, 0x24000\n"
    "v_mfma_f32_32x32x16_bf16 v[64:79], v[212:215], v[84:87], v[64:79]\n"
    "s_add_u32 s71, s59, 0x50000\n"
    "buffer_load_dwordx4 v5, s[16:19], s71 offen lds\n"
    "s_waitcnt lgkmcnt(0)\n"
    "s_barrier\n"
    "v_mfma_f32_32x32x16_bf16 v[48:63], v[216:219], v[88:91], v[48:63]\n"
    "ds_read_b64_tr_b16 v[80:81], v6 offset:16384\n"
    "ds_read_b64_tr_b16 v[82:83], v6 offset:16896\n"
    "ds_read_b64_tr_b16 v[84:85], v7 offset:16384\n"
    "ds_read_b64_tr_b16 v[86:87], v7 offset:16896\n"
    "s_add_u32 m0, s61, 0x8000\n"
    "v_mfma_f32_32x32x16_bf16 v[64:79], v[216:219], v[92:95], v[64:79]\n"
    "s_mov_b32 s71, s60\n"
    "buffer_load_dwordx4 v5, s[20:23], s71 offen lds\n"
    "s_add_u32 s71, s62, 0x20000\n"
    "buffer_load_dword v124, v2, s[28:31], s71 offen sc1\n"
    "s_waitcnt lgkmcnt(0)\n"
    "v_mfma_f32_32x32x16_bf16 v[48:63], v[220:223], v[80:83], v[48:63]\n"
    "ds_read_b64_tr_b16 v[88:89], v6 offset:24576\n"
    "ds_read_b64_tr_b16 v[90:91], v6 offset:25088\n"
    "ds_read_b64_tr_b16 v[92:93], v7 offset:24576\n"
    "ds_read_b64_tr_b16 v[94:95], v7 offset:25088\n"
    "s_add_u32 m0, s61, 0x26000\n"
    "v_mfma_f32_32x32x16_bf16 v[64:79], v[220:223], v[84:87], v[64:79]\n"
    "s_add_u32 s71, s59, 0x54000\n"
    "buffer_load_dwordx4 v5, s[16:19], s71 offen lds\n"
    "s_add_u32 m0, s61, 0xa000\n"
    "s_add_u32 s71, s60, 0x4000\n"
    "buffer_load_dwordx4 v5, s[20:23], s71 offen lds\n"
    "s_waitcnt lgkmcnt(0)\n"
    "v_mfma_f32_32x32x16_bf16 v[48:63], v[224:227], v[88:91], v[48:63]\n"
    "ds_read_b64_tr_b16 v[80:81], v6 offset:49152\n"
    "ds_read_b64_tr_b16 v[82:83], v6 offset:49664\n"
    "ds_read_b64_tr_b16 v[84:85], v7 offset:49152\n"
    "ds_read_b64_tr_b16 v[86:87], v7 offset:49664\n"
    "s_mov_b32 m0, s61\n"
    "v_mfma_f32_32x32x16_bf16 v[64:79], v[224:227], v[92:95], v[64:79]\n"
    "s_add_u32 s71, s60, 0x8000\n"
    "buffer_load_dwordx4 v5, s[20:23], s71 offen lds\n"
    "s_waitcnt lgkmcnt(0)\n"
    "v_mfma_f32_32x32x16_bf16 v[48:63], v[228:231], v[80:83], v[48:63]\n"
    "ds_read_b64_tr_b16 v[88:89], v6 offset:57344\n"
    "ds_read_b64_tr_b16 v[90:91], v6 offset:57856\n"
    "ds_read_b64_tr_b16 v[92:93], v7 offset:57344\n"
    "ds_read_b64_tr_b16 v[94:95], v7 offset:57856\n"
    "s_add_u32 m0, s61, 0x2000\n"
    "v_mfma_f32_32x32x16_bf16 v[64:79], v[228:231], v[84:87], v[64:79]\n"
    "s_add_u32 s71, s60, 0xc000\n"
    "buffer_load_dwordx4 v5, s[20:23], s71 offen lds\n"
    "s_waitcnt lgkmcnt(0)\n"
    "v_mfma_f32_32x32x16_bf16 v[48:63], v[232:235], v[88:91], v[48:63]\n"
    "v_add_u32 v6, 0x10000, v6\n"
    "ds_read_b64_tr_b16 v[80:81], v6\n"
    "ds_read_b64_tr_b16 v[82:83], v6 offset:512\n"
    "v_add_u32 v7, 0x10000, v7\n"
    "v_mfma_f32_32x32x16_bf16 v[64:79], v[232:235], v[92:95], v[64:79]\n"
    "ds_read_b64_tr_b16 v[84:85], v7\n"
    "ds_read_b64_tr_b16 v[86:87], v7 offset:512\n"
    "s_waitcnt vmcnt(22)\n"
    "s_waitcnt lgkmcnt(0)\n"
    "v_mfma_f32_32x32x16_bf16 v[48:63], v[236:239], v[80:83], v[48:63]\n"
    "ds_read_b64_tr_b16 v[88:89], v6 offset:8192\n"
    "ds_read_b64_tr_b16 v[90:91], v6 offset:8704\n"
    "ds_read_b64_tr_b16 v[92:93], v7 offset:8192\n"
    "ds_read_b64_tr_b16 v[94:95], v7 offset:8704\n"
    "v_mfma_f32_32x32x16_bf16 v[64:79], v[236:239], v[84:87], v[64:79]\n"
    "s_waitcnt vmcnt(21)\n"
    "s_waitcnt lgkmcnt(0)\n"
    "v_mfma_f32_32x32x16_bf16 v[48:63], v[240:243], v[88:91], v[48:63]\n"
    "ds_read_b64_tr_b16 v[80:81], v6 offset:16384\n"
    "ds_read_b64_tr_b16 v[82:83], v6 offset:16896\n"
    "ds_read_b64_tr_b16 v[84:85], v7 offset:16384\n"
    "ds_read_b64_tr_b16 v[86:87], v7 offset:16896\n"
    "v_mfma_f32_32x32x16_bf16 v[64:79], v[240:243], v[92:95], v[64:79]\n"
    "s_waitcnt vmcnt(20)\n"
    "s_waitcnt lgkmcnt(0)\n"
    "v_mfma_f32_32x32x16_bf16 v[48:63], v[244:247], v[80:83], v[48:63]\n"
    "ds_read_b64_tr_b16 v[88:89], v6 offset:24576\n"
    "ds_read_b64_tr_b16 v[90:91], v6 offset:25088\n"
    "ds_read_b64_tr_b16 v[92:93], v7 offset:24576\n"
    "ds_read_b64_tr_b16 v[94:95], v7 offset:25088\n"
    "v_mfma_f32_32x32x16_bf16 v[64:79], v[244:247], v[84:87], v[64:79]\n"
    "s_waitcnt vmcnt(7) lgkmcnt(0)\n"
    "s_barrier\n"
    "v_mfma_f32_32x32x16_bf16 v[48:63], v[248:251], v[88:91], v[48:63]\n"
    "ds_read_b64_tr_b16 v[80:81], v6 offset:49152\n"
    "ds_read_b64_tr_b16 v[82:83], v6 offset:49664\n"
    "ds_read_b64_tr_b16 v[84:85], v7 offset:49152\n"
    "ds_read_b64_tr_b16 v[86:87], v7 offset:49664\n"
    "s_add_u32 m0, s61, 0x12000\n"
    "v_mfma_f32_32x32x16_bf16 v[64:79], v[248:251], v[92:95], v[64:79]\n"
    "s_add_u32 s71, s60, 0x10000\n"
    "buffer_load_dwordx4 v5, s[20:23], s71 offen lds\n"
    "s_waitcnt vmcnt(5)\n"
    "v_cmp_ne_u32 vcc, v22, v124\n"
    "s_cbranch_vccnz .Lk_spin_11\n"
    ".Lk_ok_12:\n"
    "s_waitcnt lgkmcnt(0)\n"
    "v_mfma_f32_32x32x16_bf16 v[48:63], v[252:255], v[80:83], v[48:63]\n"
    "ds_read_b64_tr_b16 v[88:89], v6 offset:57344\n"
    "ds_read_b64_tr_b16 v[90:91], v6 offset:57856\n"
    "ds_read_b64_tr_b16 v[92:93], v7 offset:57344\n"
    "ds_read_b64_tr_b16 v[94:95], v7 offset:57856\n"
    "s_add_u32 m0, s61, 0x14000\n"
    "s_add_u32 s71, s60, 0x14000\n"
    "v_mfma_f32_32x32x16_bf16 v[64:79], v[252:255], v[84:87], v[64:79]\n"
    "buffer_load_dwordx4 v5, s[20:23], s71 offen lds\n"
    "s_mul_i32 s68, s36, 0x5000\n"
    "s_sub_u32 s68, s58, s68\n"
    "s_add_u32 s68, s68, 0x1c000\n"
    "buffer_load_dwordx4 v[172:175], v1, s[28:31], s68 offen sc1\n"
    "buffer_load_dwordx4 v[176:179], v1, s[28:31], s68 offen offset:1024 sc1\n"
    "s_waitcnt lgkmcnt(0)\n"
    "v_mfma_f32_32x32x16_bf16 v[48:63], v[96:99], v[88:91], v[48:63]\n"
    "v_add_u32 v6, 0x10000, v6\n"
    "ds_read_b64_tr_b16 v[80:81], v6\n"
    "ds_read_b64_tr_b16 v[82:83], v6 offset:512\n"
    "v_add_u32 v7, 0x10000, v7\n"
    "s_add_u32 m0, s61, 0x16000\n"
    "s_add_u32 s71, s60, 0x18000\n"
    "v_mfma_f32_32x32x16_bf16 v[64:79], v[96:99], v[92:95], v[64:79]\n"
    "ds_read_b64_tr_b16 v[84:85], v7\n"
    "ds_read_b64_tr_b16 v[86:87], v7 offset:512\n"
    "buffer_load_dwordx4 v5, s[20:23], s71 offen lds\n"
    "buffer_load_dwordx4 v[180:183], v1, s[28:31], s68 offen offset:2048 sc1\n"
    "buffer_load_dwordx4 v[184:187], v1, s[28:31], s68 offen offset:3072 sc1\n"
    "s_waitcnt lgkmcnt(0)\n"
    "v_mfma_f32_32x32x16_bf16 v[48:63], v[100:103], v[80:83], v[48:63]\n"
    "ds_read_b64_tr_b16 v[88:89], v6 offset:8192\n"
    "ds_read_b64_tr_b16 v[90:91], v6 offset:8704\n"
    "ds_read_b64_tr_b16 v[92:93], v7 offset:8192\n"
    "ds_read_b64_tr_b16 v[94:95], v7 offset:8704\n"
    "s_mul_i32 s68, s36, 0x5000\n"
    "v_mfma_f32_32x32x16_bf16 v[64:79], v[100:103], v[84:87], v[64:79]\n"
    "s_sub_u32 s68, s58, s68\n"
    "s_add_u32 s68, s68, 0x1d000\n"
    "buffer_load_dwordx4 v[188:191], v1, s[28:31], s68 offen sc1\n"
    "buffer_load_dwordx4 v[192:195], v1, s[28:31], s68 offen offset:1024 sc1\n"
    "s_waitcnt vmcnt(12) lgkmcnt(0)\n"
    "s_barrier\n"
    "v_mfma_f32_32x32x16_bf16 v[48:63], v[104:107], v[88:91], v[48:63]\n"
    "ds_read_b64_tr_b16 v[80:81], v6 offset:16384\n"
    "ds_read_b64_tr_b16 v[82:83], v6 offset:16896\n"
    "ds_read_b64_tr_b16 v[84:85], v7 offset:16384\n"
    "ds_read_b64_tr_b16 v[86:87], v7 offset:16896\n"
    "s_add_u32 m0, s61, 0x1c000\n"
    "v_mfma_f32_32x32x16_bf16 v[64:79], v[104:107], v[92:95], v[64:79]\n"
    "s_add_u32 s71, s60, 0x1c000\n"
    "buffer_load_dwordx4 v5, s[20:23], s71 offen lds\n"
    "buffer_load_dwordx4 v[196:199], v1, s[28:31], s68 offen offset:2048 sc1\n"
    "buffer_load_dwordx4 v[200:203], v1, s[28:31], s68 offen offset:3072 sc1\n"
    "s_waitcnt lgkmcnt(0)\n"
    "v_mfma_f32_32x32x16_bf16 v[48:63], v[108:111], v[80:83], v[48:63]\n"
    "ds_read_b64_tr_b16 v[88:89], v6 offset:24576\n"
    "ds_read_b64_tr_b16 v[90:91], v6 offset:25088\n"
    "ds_read_b64_tr_b16 v[92:93], v7 offset:24576\n"
    "ds_read_b64_tr_b16 v[94:95], v7 offset:25088\n"
    "v_lshrrev_b32 v8, 4, v0\n"
    "v_bfe_u32 v23, v0, 3, 1\n"
    "v_mfma_f32_32x32x16_bf16 v[64:79], v[108:111], v[84:87], v[64:79]\n"
    "v_lshl_add_u32 v8, v23, 2, v8\n"
    "v_and_b32 v23, 7, v0\n"
    "v_lshlrev_b32 v23, 4, v23\n"
    "v_lshl_or_b32 v8, v8, 10, v23\n"
    "buffer_load_dwordx4 v[40:43], v8, s[4:7], s63 offen\n"
    "s_waitcnt lgkmcnt(0)\n"
    "v_mfma_f32_32x32x16_bf16 v[48:63], v[112:115], v[88:91], v[48:63]\n"
    "v_add_u32 v6, 0xffff0000, v6\n"
    "ds_read_b64_tr_b16 v[80:81], v6 offset:32768\n"
    "ds_read_b64_tr_b16 v[82:83], v6 offset:33280\n"
    "v_add_u32 v7, 0xffff0000, v7\n"
    "v_mfma_f32_32x32x16_bf16 v[64:79], v[112:115], v[92:95], v[64:79]\n"
    "ds_read_b64_tr_b16 v[84:85], v7 offset:32768\n"
    "ds_read_b64_tr_b16 v[86:87], v7 offset:33280\n"
    "s_waitcnt lgkmcnt(0)\n"
    "v_mfma_f32_32x32x16_bf16 v[48:63], v[116:119], v[80:83], v[48:63]\n"
    "ds_read_b64_tr_b16 v[88:89], v6 offset:40960\n"
    "ds_read_b64_tr_b16 v[90:91], v6 offset:41472\n"
    "ds_read_b64_tr_b16 v[92:93], v7 offset:40960\n"
    "ds_read_b64_tr_b16 v[94:95], v7 offset:41472\n"
    "v_lshrrev_b32 v9, 4, v0\n"
    "v_lshlrev_b32 v9, 8, v9\n"
    "v_bfe_u32 v23, v0, 3, 1\n"
    "v_lshl_add_u32 v9, v23, 7, v9\n"
    "v_bfe_u32 v23, v0, 2, 1\n"
    "v_mfma_f32_32x32x16_bf16 v[64:79], v[116:119], v[84:87], v[64:79]\n"
    "v_mul_u32_u24 v23, 0x1010, v23\n"
    "v_add_u32 v9, v9, v23\n"
    "v_and_b32 v23, 3, v0\n"
    "v_lshl_add_u32 v9, v23, 5, v9\n"
    "s_mul_i32 s70, s37, 0x8080\n"
    "s_lshl_b32 s69, s36, 10\n"
    "s_add_u32 s70, s70, s69\n"
    "v_add_u32 v9, s70, v9\n"
    "s_waitcnt vmcnt(13) lgkmcnt(0)\n"
    "s_barrier\n"
    "v_mfma_f32_32x32x16_bf16 v[48:63], v[120:123], v[88:91], v[48:63]\n"
    "v_add_u32 v6, 0xffff0000, v6\n"
    "ds_read_b64_tr_b16 v[80:81], v6 offset:32768\n"
    "ds_read_b64_tr_b16 v[82:83], v6 offset:33280\n"
    "v_add_u32 v7, 0xffff0000, v7\n"
    "s_or_b32 s70, s32, s33\n"
    "s_cmp_eq_u32 s70, 0\n"
    "s_cbranch_scc1 .Lk_eug_14\n"
    ".Lk_eu_13:\n"
    "v_mfma_f32_32x32x16_bf16 v[64:79], v[120:123], v[92:95], v[64:79]\n"
    "ds_read_b64_tr_b16 v[84:85], v7 offset:32768\n"
    "ds_read_b64_tr_b16 v[86:87], v7 offset:33280\n"
    "s_waitcnt vmcnt(10)\n"
    "s_waitcnt lgkmcnt(0)\n"
    "v_mfma_f32_32x32x16_bf16 v[48:63], v[172:175], v[80:83], v[48:63]\n"
    "ds_read_b64_tr_b16 v[88:89], v6 offset:40960\n"
    "ds_read_b64_tr_b16 v[90:91], v6 offset:41472\n"
    "ds_read_b64_tr_b16 v[92:93], v7 offset:40960\n"
    "ds_read_b64_tr_b16 v[94:95], v7 offset:41472\n"
    "v_mfma_f32_32x32x16_bf16 v[64:79], v[172:175], v[84:87], v[64:79]\n"
    "s_waitcnt vmcnt(9)\n"
    "s_waitcnt lgkmcnt(0)\n"
    "v_mfma_f32_32x32x16_bf16 v[48:63], v[176:179], v[88:91], v[48:63]\n"
    "ds_read_b64_tr_b16 v[80:81], v6\n"
    "ds_read_b64_tr_b16 v[82:83], v6 offset:512\n"
    "ds_read_b64_tr_b16 v[84:85], v7\n"
    "ds_read_b64_tr_b16 v[86:87], v7 offset:512\n"
    "v_mfma_f32_32x32x16_bf16 v[64:79], v[176:179], v[92:95], v[64:79]\n"
    "s_waitcnt vmcnt(7)\n"
    "s_waitcnt lgkmcnt(0)\n"
    "v_mfma_f32_32x32x16_bf16 v[48:63], v[180:183], v[80:83], v[48:63]\n"
    "ds_read_b64_tr_b16 v[88:89], v6 offset:8192\n"
    "ds_read_b64_tr_b16 v[90:91], v6 offset:8704\n"
    "ds_read_b64_tr_b16 v[92:93], v7 offset:8192\n"
    "ds_read_b64_tr_b16 v[94:95], v7 offset:8704\n"
    "v_mfma_f32_32x32x16_bf16 v[64:79], v[180:183], v[84:87], v[64:79]\n"
    "s_waitcnt vmcnt(3) lgkmcnt(0)\n"
    "s_barrier\n"
    "v_mfma_f32_32x32x16_bf16 v[48:63], v[184:187], v[88:91], v[48:63]\n"
    "v_add_u32 v6, 0x10000, v6\n"
    "ds_read_b64_tr_b16 v[80:81], v6 offset:8192\n"
    "ds_read_b64_tr_b16 v[82:83], v6 offset:8704\n"
    "v_add_u32 v7, 0x10000, v7\n"
    "v_mfma_f32_32x32x16_bf16 v[64:79], v[184:187], v[92:95], v[64:79]\n"
    "ds_read_b64_tr_b16 v[84:85], v7 offset:8192\n"
    "ds_read_b64_tr_b16 v[86:87], v7 offset:8704\n"
    "s_waitcnt lgkmcnt(0)\n"
    "v_mfma_f32_32x32x16_bf16 v[48:63], v[188:191], v[80:83], v[48:63]\n"
    "ds_read_b64_tr_b16 v[88:89], v6 offset:16384\n"
    "ds_read_b64_tr_b16 v[90:91], v6 offset:16896\n"
    "ds_read_b64_tr_b16 v[92:93], v7 offset:16384\n"
    "ds_read_b64_tr_b16 v[94:95], v7 offset:16896\n"
    "v_mfma_f32_32x32x16_bf16 v[64:79], v[188:191], v[84:87], v[64:79]\n"
    "s_waitcnt lgkmcnt(0)\n"
    "v_mfma_f32_32x32x16_bf16 v[48:63], v[192:195], v[88:91], v[48:63]\n"
    "ds_read_b64_tr_b16 v[80:81], v6 offset:24576\n"
    "ds_read_b64_tr_b16 v[82:83], v6 offset:25088\n"
    "ds_read_b64_tr_b16 v[84:85], v7 offset:24576\n"
    "ds_read_b64_tr_b16 v[86:87], v7 offset:25088\n"
    "v_mfma_f32_32x32x16_bf16 v[64:79], v[192:195], v[92:95], v[64:79]\n"
    "s_waitcnt vmcnt(2)\n"
    "s_waitcnt lgkmcnt(0)\n"
    "v_mfma_f32_32x32x16_bf16 v[48:63], v[196:199], v[80:83], v[48:63]\n"
    "ds_read_b64_tr_b16 v[88:89], v6 offset:49152\n"
    "ds_read_b64_tr_b16 v[90:91], v6 offset:49664\n"
    "ds_read_b64_tr_b16 v[92:93], v7 offset:49152\n"
    "ds_read_b64_tr_b16 v[94:95], v7 offset:49664\n"
    "v_mfma_f32_32x32x16_bf16 v[64:79], v[196:199], v[84:87], v[64:79]\n"
    "s_waitcnt vmcnt(1)\n"
    "s_waitcnt lgkmcnt(0)\n"
    "v_mfma_f32_32x32x16_bf16 v[48:63], v[200:203], v[88:91], v[48:63]\n"
    "v_mfma_f32_32x32x16_bf16 v[64:79], v[200:203], v[92:95], v[64:79]\n"
    "s_mul_i32 s70, s33, 0x2020\n"
    "s_mov_b32 m0, s70\n"
    "s_nop 8\n"
    "ds_write_addtid_b32 v48 offset:0\n"
    "ds_write_addtid_b32 v49 offset:256\n"
    "ds_write_addtid_b32 v50 offset:512\n"
    "ds_write_addtid_b32 v51 offset:768\n"
    "ds_write_addtid_b32 v52 offset:1024\n"
    "ds_write_addtid_b32 v53 offset:1280\n"
    "ds_write_addtid_b32 v54 offset:1536\n"
    "ds_write_addtid_b32 v55 offset:1792\n"
    "ds_write_addtid_b32 v56 offset:2048\n"
    "ds_write_addtid_b32 v57 offset:2304\n"
    "ds_write_addtid_b32 v58 offset:2560\n"
    "ds_write_addtid_b32 v59 offset:2816\n"
    "ds_write_addtid_b32 v60 offset:3072\n"
    "ds_write_addtid_b32 v61 offset:3328\n"
    "ds_write_addtid_b32 v62 offset:3584\n"
    "ds_write_addtid_b32 v63 offset:3840\n"
    "ds_write_addtid_b32 v64 offset:4112\n"
    "ds_write_addtid_b32 v65 offset:4368\n"
    "ds_write_addtid_b32 v66 offset:4624\n"
    "ds_write_addtid_b32 v67 offset:4880\n"
    "ds_write_addtid_b32 v68 offset:5136\n"
    "ds_write_addtid_b32 v69 offset:5392\n"
    "ds_write_addtid_b32 v70 offset:5648\n"
    "ds_write_addtid_b32 v71 offset:5904\n"
    "ds_write_addtid_b32 v72 offset:6160\n"
    "ds_write_addtid_b32 v73 offset:6416\n"
    "ds_write_addtid_b32 v74 offset:6672\n"
    "ds_write_addtid_b32 v75 offset:6928\n"
    "ds_write_addtid_b32 v76 offset:7184\n"
    "ds_write_addtid_b32 v77 offset:7440\n"
    "ds_write_addtid_b32 v78 offset:7696\n"
    "ds_write_addtid_b32 v79 offset:7952\n"
    "s_waitcnt lgkmcnt(0)\n"
    "s_barrier\n"
    "ds_read_b128 v[128:131], v9\n"
    "ds_read_b128 v[132:135], v9 offset:16\n"
    "ds_read_b128 v[136:139], v9 offset:8224\n"
    "ds_read_b128 v[140:143], v9 offset:8240\n"
    "ds_read_b128 v[144:147], v9 offset:16448\n"
    "ds_read_b128 v[148:151], v9 offset:16464\n"
    "ds_read_b128 v[152:155], v9 offset:24672\n"
    "ds_read_b128 v[156:159], v9 offset:24688\n"
    "s_waitcnt vmcnt(0)\n"
    "v_lshlrev_b32 v160, 16, v40\n"
    "v_and_b32 v161, 0xffff0000, v40\n"
    "v_lshlrev_b32 v162, 16, v41\n"
    "v_and_b32 v163, 0xffff0000, v41\n"
    "v_lshlrev_b32 v164, 16, v42\n"
    "v_and_b32 v165, 0xffff0000, v42\n"
    "v_lshlrev_b32 v166, 16, v43\n"
    "v_and_b32 v167, 0xffff0000, v43\n"
    "s_waitcnt lgkmcnt(6)\n"
    "v_pk_add_f32 v[160:161], v[160:161], v[128:129]\n"
    "v_pk_add_f32 v[162:163], v[162:163], v[130:131]\n"
    "v_pk_add_f32 v[164:165], v[164:165], v[132:133]\n"
    "v_pk_add_f32 v[166:167], v[166:167], v[134:135]\n"
    "s_waitcnt lgkmcnt(4)\n"
    "v_pk_add_f32 v[160:161], v[160:161], v[136:137]\n"
    "v_pk_add_f32 v[162:163], v[162:163], v[138:139]\n"
    "v_pk_add_f32 v[164:165], v[164:165], v[140:141]\n"
    "v_pk_add_f32 v[166:167], v[166:167], v[142:143]\n"
    "s_waitcnt lgkmcnt(2)\n"
    "v_pk_add_f32 v[160:161], v[160:161], v[144:145]\n"
    "v_pk_add_f32 v[162:163], v[162:163], v[146:147]\n"
    "v_pk_add_f32 v[164:165], v[164:165], v[148:149]\n"
    "v_pk_add_f32 v[166:167], v[166:167], v[150:151]\n"
    "s_waitcnt lgkmcnt(0)\n"
    "v_pk_add_f32 v[160:161], v[160:161], v[152:153]\n"
    "v_pk_add_f32 v[162:163], v[162:163], v[154:155]\n"
    "v_pk_add_f32 v[164:165], v[164:165], v[156:157]\n"
    "v_pk_add_f32 v[166:167], v[166:167], v[158:159]\n"
    "v_cvt_pk_bf16_f32 v40, v160, v161\n"
    "v_cvt_pk_bf16_f32 v41, v162, v163\n"
    "v_cvt_pk_bf16_f32 v42, v164, v165\n"
    "v_cvt_pk_bf16_f32 v43, v166, v167\n"
    "buffer_store_dwordx4 v[40:43], v8, s[24:27], s63 offen\n"
    "s_endpgm\n"
    "s_nop 7\n"
    ".Lk_spin_1:\n"
    "s_sleep 1\n"
    "v_and_b32 v23, 0xf0, v1\n"
    "buffer_load_dwordx4 v[40:43], v23, s[28:31], s55 offen sc1\n"
    "buffer_load_dwordx4 v[44:47], v23, s[28:31], s55 offen offset:256 sc1\n"
    "s_waitcnt vmcnt(0)\n"
    "v_xor_b32 v23, v22, v41\n"
    "v_xor_b32 v124, v22, v43\n"
    "v_or_b32 v23, v23, v124\n"
    "v_xor_b32 v124, v22, v45\n"
    "v_or_b32 v23, v23, v124\n"
    "v_xor_b32 v124, v22, v47\n"
    "v_or_b32 v23, v23, v124\n"
    "v_cmp_ne_u32 vcc, 0, v23\n"
    "s_add_u32 s74, s74, 1\n"
    "s_cbranch_vccz .Lk_ok_2\n"
    "s_cmp_lt_u32 s74, 0x20000\n"
    "s_cbranch_scc1 .Lk_spin_1\n"
    "s_branch .Lk_ok_2\n"
    "s_nop 7\n"
    ".Lk_spin_3:\n"
    "s_sleep 1\n"
    "s_mov_b32 s71, 0x8000\n"
    "buffer_load_dword v124, v125, s[28:31], s71 offen sc1\n"
    "s_waitcnt vmcnt(0)\n"
    "v_cmp_ne_u32 vcc, v22, v124\n"
    "s_add_u32 s74, s74, 1\n"
    "s_cbranch_vccz .Lk_ok_4\n"
    "s_cmp_lt_u32 s74, 0x20000\n"
    "s_cbranch_scc1 .Lk_spin_3\n"
    "s_branch .Lk_ok_4\n"
    "s_nop 7\n"
    ".Lk_spin_5:\n"
    "s_sleep 1\n"
    "s_mov_b32 s71, 0x8000\n"
    "buffer_load_dword v124, v125, s[28:31], s71 offen sc1\n"
    "s_waitcnt vmcnt(0)\n"
    "v_cmp_ne_u32 vcc, v22, v124\n"
    "s_add_u32 s74, s74, 1\n"
    "s_cbranch_vccz .Lk_ok_6\n"
    "s_cmp_lt_u32 s74, 0x20000\n"
    "s_cbranch_scc1 .Lk_spin_5\n"
    "s_branch .Lk_ok_6\n"
    "s_nop 7\n"
    ".Lk_spin_7:\n"
    "s_sleep 1\n"
    "s_add_u32 s71, s62, 0x10000\n"
    "buffer_load_dword v124, v2, s[28:31], s71 offen sc1\n"
    "s_waitcnt vmcnt(0)\n"
    "v_cmp_ne_u32 vcc, v22, v124\n"
    "s_add_u32 s74, s74, 1\n"
    "s_cbranch_vccz .Lk_ok_8\n"
    "s_cmp_lt_u32 s74, 0x20000\n"
    "s_cbranch_scc1 .Lk_spin_7\n"
    "s_branch .Lk_ok_8\n"
    "s_nop 7\n"
    ".Lk_slow:\n"
    "v_sub_f32 v128, s75, v144\n"
    "v_sub_f32 v129, s75, v145\n"
    "v_sub_f32 v130, s75, v146\n"
    "v_sub_f32 v131, s75, v147\n"
    "v_sub_f32 v132, s75, v148\n"
    "v_sub_f32 v133, s75, v149\n"
    "v_sub_f32 v134, s75, v150\n"
    "v_sub_f32 v135, s75, v151\n"
    "v_sub_f32 v136, s75, v152\n"
    "v_sub_f32 v137, s75, v153\n"
    "v_sub_f32 v138, s75, v154\n"
    "v_sub_f32 v139, s75, v155\n"
    "v_sub_f32 v140, s75, v156\n"
    "v_sub_f32 v141, s75, v157\n"
    "v_sub_f32 v142, s75, v158\n"
    "v_sub_f32 v143, s75, v159\n"
    "v_mov_b32 v169, 0xff61b1e6\n"
    "s_waitcnt lgkmcnt(0)\n"
    "v_mfma_f32_32x32x16_bf16 v[48:63], v[32:35], v[28:31], v[128:143]\n"
    "s_nop 11\n"
    "v_max3_f32 v169, v48, v49, v169\n"
    "v_max3_f32 v169, v50, v51, v169\n"
    "v_max3_f32 v169, v52, v53, v169\n"
    "v_max3_f32 v169, v54, v55, v169\n"
    "v_max3_f32 v169, v56, v57, v169\n"
    "v_max3_f32 v169, v58, v59, v169\n"
    "v_max3_f32 v169, v60, v61, v169\n"
    "v_max3_f32 v169, v62, v63, v169\n"
    "s_nop 15\n"
    "s_cmp_lt_u32 s65, 1\n"
    "s_cbranch_scc1 .Lk_pe_15\n"
    "ds_read_b128 v[80:83], v19 offset:28672\n"
    "s_waitcnt lgkmcnt(0)\n"
    "v_mfma_f32_32x32x16_bf16 v[48:63], v[80:83], v[28:31], 0\n"
    "s_nop 11\n"
    "v_max3_f32 v169, v48, v49, v169\n"
    "v_max3_f32 v169, v50, v51, v169\n"
    "v_max3_f32 v169, v52, v53, v169\n"
    "v_max3_f32 v169, v54, v55, v169\n"
    "v_max3_f32 v169, v56, v57, v169\n"
    "v_max3_f32 v169, v58, v59, v169\n"
    "v_max3_f32 v169, v60, v61, v169\n"
    "v_max3_f32 v169, v62, v63, v169\n"
    "s_nop 15\n"
    "s_cmp_lt_u32 s65, 2\n"
    "s_cbranch_scc1 .Lk_pe_15\n"
    "ds_read_b128 v[80:83], v19 offset:24576\n"
    "s_waitcnt lgkmcnt(0)\n"
    "v_mfma_f32_32x32x16_bf16 v[48:63], v[80:83], v[28:31], 0\n"
    "s_nop 11\n"
    "v_max3_f32 v169, v48, v49, v169\n"
    "v_max3_f32 v169, v50, v51, v169\n"
    "v_max3_f32 v169, v52, v53, v169\n"
    "v_max3_f32 v169, v54, v55, v169\n"
    "v_max3_f32 v169, v56, v57, v169\n"
    "v_max3_f32 v169, v58, v59, v169\n"
    "v_max3_f32 v169, v60, v61, v169\n"
    "v_max3_f32 v169, v62, v63, v169\n"
    "s_nop 15\n"
    "s_cmp_lt_u32 s65, 3\n"
    "s_cbranch_scc1 .Lk_pe_15\n"
    "ds_read_b128 v[80:83], v19 offset:20480\n"
    "s_waitcnt lgkmcnt(0)\n"
    "v_mfma_f32_32x32x16_bf16 v[48:63], v[80:83], v[28:31], 0\n"
    "s_nop 11\n"
    "v_max3_f32 v169, v48, v49, v169\n"
    "v_max3_f32 v169, v50, v51, v169\n"
    "v_max3_f32 v169, v52, v53, v169\n"
    "v_max3_f32 v169, v54, v55, v169\n"
    "v_max3_f32 v169, v56, v57, v169\n"
    "v_max3_f32 v169, v58, v59, v169\n"
    "v_max3_f32 v169, v60, v61, v169\n"
    "v_max3_f32 v169, v62, v63, v169\n"
    "s_nop 15\n"
    "s_cmp_lt_u32 s65, 4\n"
    "s_cbranch_scc1 .Lk_pe_15\n"
    "ds_read_b128 v[80:83], v19 offset:16384\n"
    "s_waitcnt lgkmcnt(0)\n"
    "v_mfma_f32_32x32x16_bf16 v[48:63], v[80:83], v[28:31], 0\n"
    "s_nop 11\n"
    "v_max3_f32 v169, v48, v49, v169\n"
    "v_max3_f32 v169, v50, v51, v169\n"
    "v_max3_f32 v169, v52, v53, v169\n"
    "v_max3_f32 v169, v54, v55, v169\n"
    "v_max3_f32 v169, v56, v57, v169\n"
    "v_max3_f32 v169, v58, v59, v169\n"
    "v_max3_f32 v169, v60, v61, v169\n"
    "v_max3_f32 v169, v62, v63, v169\n"
    "s_nop 15\n"
    "s_cmp_lt_u32 s65, 5\n"
    "s_cbranch_scc1 .Lk_pe_15\n"
    "ds_read_b128 v[80:83], v19 offset:12288\n"
    "s_waitcnt lgkmcnt(0)\n"
    "v_mfma_f32_32x32x16_bf16 v[48:63], v[80:83], v[28:31], 0\n"
    "s_nop 11\n"
    "v_max3_f32 v169, v48, v49, v169\n"
    "v_max3_f32 v169, v50, v51, v169\n"
    "v_max3_f32 v169, v52, v53, v169\n"
    "v_max3_f32 v169, v54, v55, v169\n"
    "v_max3_f32 v169, v56, v57, v169\n"
    "v_max3_f32 v169, v58, v59, v169\n"
    "v_max3_f32 v169, v60, v61, v169\n"
    "v_max3_f32 v169, v62, v63, v169\n"
    "s_nop 15\n"
    "s_cmp_lt_u32 s65, 6\n"
    "s_cbranch_scc1 .Lk_pe_15\n"
    "ds_read_b128 v[80:83], v19 offset:8192\n"
    "s_waitcnt lgkmcnt(0)\n"
    "v_mfma_f32_32x32x16_bf16 v[48:63], v[80:83], v[28:31], 0\n"
    "s_nop 11\n"
    "v_max3_f32 v169, v48, v49, v169\n"
    "v_max3_f32 v169, v50, v51, v169\n"
    "v_max3_f32 v169, v52, v53, v169\n"
    "v_max3_f32 v169, v54, v55, v169\n"
    "v_max3_f32 v169, v56, v57, v169\n"
    "v_max3_f32 v169, v58, v59, v169\n"
    "v_max3_f32 v169, v60, v61, v169\n"
    "v_max3_f32 v169, v62, v63, v169\n"
    "s_nop 15\n"
    "s_cmp_lt_u32 s65, 7\n"
    "s_cbranch_scc1 .Lk_pe_15\n"
    "ds_read_b128 v[80:83], v19 offset:4096\n"
    "s_waitcnt lgkmcnt(0)\n"
    "v_mfma_f32_32x32x16_bf16 v[48:63], v[80:83], v[28:31], 0\n"
    "s_nop 11\n"
    "v_max3_f32 v169, v48, v49, v169\n"
    "v_max3_f32 v169, v50, v51, v169\n"
    "v_max3_f32 v169, v52, v53, v169\n"
    "v_max3_f32 v169, v54, v55, v169\n"
    "v_max3_f32 v169, v56, v57, v169\n"
    "v_max3_f32 v169, v58, v59, v169\n"
    "v_max3_f32 v169, v60, v61, v169\n"
    "v_max3_f32 v169, v62, v63, v169\n"
    "s_nop 15\n"
    "s_cmp_lt_u32 s65, 8\n"
    "s_cbranch_scc1 .Lk_pe_15\n"
    "ds_read_b128 v[80:83], v19\n"
    "s_waitcnt lgkmcnt(0)\n"
    "v_mfma_f32_32x32x16_bf16 v[48:63], v[80:83], v[28:31], v[144:159]\n"
    "s_nop 11\n"
    "v_max3_f32 v169, v48, v49, v169\n"
    "v_max3_f32 v169, v50, v51, v169\n"
    "v_max3_f32 v169, v52, v53, v169\n"
    "v_max3_f32 v169, v54, v55, v169\n"
    "v_max3_f32 v169, v56, v57, v169\n"
    "v_max3_f32 v169, v58, v59, v169\n"
    "v_max3_f32 v169, v60, v61, v169\n"
    "v_max3_f32 v169, v62, v63, v169\n"
    "s_nop 15\n"
    ".Lk_pe_15:\n"
    "s_nop 15\n"
    "v_mov_b32 v170, v169\n"
    "s_nop 1\n"
    "v_permlane32_swap_b32 v169, v170\n"
    "v_max_f32 v169, v169, v170\n"
    "ds_read_b64_tr_b16 v[88:89], v160\n"
    "ds_read_b64_tr_b16 v[90:91], v160 offset:256\n"
    "ds_read_b64_tr_b16 v[92:93], v160 offset:512\n"
    "ds_read_b64_tr_b16 v[94:95], v160 offset:768\n"
    "s_waitcnt lgkmcnt(0)\n"
    "v_mfma_f32_32x32x16_bf16 v[48:63], v[32:35], v[28:31], v[128:143]\n"
    "s_nop 11\n"
    "v_sub_f32 v48, v48, v169\n"
    "v_sub_f32 v49, v49, v169\n"
    "v_sub_f32 v50, v50, v169\n"
    "v_sub_f32 v51, v51, v169\n"
    "v_sub_f32 v52, v52, v169\n"
    "v_sub_f32 v53, v53, v169\n"
    "v_sub_f32 v54, v54, v169\n"
    "v_sub_f32 v55, v55, v169\n"
    "v_sub_f32 v56, v56, v169\n"
    "v_sub_f32 v57, v57, v169\n"
    "v_sub_f32 v58, v58, v169\n"
    "v_sub_f32 v59, v59, v169\n"
    "v_sub_f32 v60, v60, v169\n"
    "v_sub_f32 v61, v61, v169\n"
    "v_sub_f32 v62, v62, v169\n"
    "v_sub_f32 v63, v63, v169\n"
    "v_exp_f32 v48, v48\n"
    "v_exp_f32 v49, v49\n"
    "v_exp_f32 v50, v50\n"
    "v_exp_f32 v51, v51\n"
    "v_exp_f32 v52, v52\n"
    "v_exp_f32 v53, v53\n"
    "v_exp_f32 v54, v54\n"
    "v_exp_f32 v55, v55\n"
    "v_exp_f32 v56, v56\n"
    "v_exp_f32 v57, v57\n"
    "v_exp_f32 v58, v58\n"
    "v_exp_f32 v59, v59\n"
    "v_exp_f32 v60, v60\n"
    "v_exp_f32 v61, v61\n"
    "v_exp_f32 v62, v62\n"
    "v_exp_f32 v63, v63\n"
    "v_cvt_pk_bf16_f32 v40, v48, v49\n"
    "v_cvt_pk_bf16_f32 v41, v50, v51\n"
    "v_cvt_pk_bf16_f32 v42, v52, v53\n"
    "v_cvt_pk_bf16_f32 v43, v54, v55\n"
    "v_cvt_pk_bf16_f32 v44, v56, v57\n"
    "v_cvt_pk_bf16_f32 v45, v58, v59\n"
    "v_cvt_pk_bf16_f32 v46, v60, v61\n"
    "v_cvt_pk_bf16_f32 v47, v62, v63\n"
    "v_mfma_f32_32x32x16_bf16 v[64:79], v[88:91], v[40:43], 0\n"
    "s_nop 0\n"
    "v_mfma_f32_32x32x16_bf16 v[64:79], v[92:95], v[44:47], v[64:79]\n"
    "s_nop 15\n"
    "s_cmp_lt_u32 s65, 1\n"
    "s_cbranch_scc1 .Lk_pe_16\n"
    "ds_read_b128 v[80:83], v19 offset:28672\n"
    "ds_read_b64_tr_b16 v[88:89], v161\n"
    "ds_read_b64_tr_b16 v[90:91], v161 offset:256\n"
    "ds_read_b64_tr_b16 v[92:93], v161 offset:512\n"
    "ds_read_b64_tr_b16 v[94:95], v161 offset:768\n"
    "s_waitcnt lgkmcnt(0)\n"
    "v_mfma_f32_32x32x16_bf16 v[48:63], v[80:83], v[28:31], 0\n"
    "s_nop 11\n"
    "v_sub_f32 v48, v48, v169\n"
    "v_sub_f32 v49, v49, v169\n"
    "v_sub_f32 v50, v50, v169\n"
    "v_sub_f32 v51, v51, v169\n"
    "v_sub_f32 v52, v52, v169\n"
    "v_sub_f32 v53, v53, v169\n"
    "v_sub_f32 v54, v54, v169\n"
    "v_sub_f32 v55, v55, v169\n"
    "v_sub_f32 v56, v56, v169\n"
    "v_sub_f32 v57, v57, v169\n"
    "v_sub_f32 v58, v58, v169\n"
    "v_sub_f32 v59, v59, v169\n"
    "v_sub_f32 v60, v60, v169\n"
    "v_sub_f32 v61, v61, v169\n"
    "v_sub_f32 v62, v62, v169\n"
    "v_sub_f32 v63, v63, v169\n"
    "v_exp_f32 v48, v48\n"
    "v_exp_f32 v49, v49\n"
    "v_exp_f32 v50, v50\n"
    "v_exp_f32 v51, v51\n"
    "v_exp_f32 v52, v52\n"
    "v_exp_f32 v53, v53\n"
    "v_exp_f32 v54, v54\n"
    "v_exp_f32 v55, v55\n"
    "v_exp_f32 v56, v56\n"
    "v_exp_f32 v57, v57\n"
    "v_exp_f32 v58, v58\n"
    "v_exp_f32 v59, v59\n"
    "v_exp_f32 v60, v60\n"
    "v_exp_f32 v61, v61\n"
    "v_exp_f32 v62, v62\n"
    "v_exp_f32 v63, v63\n"
    "v_cvt_pk_bf16_f32 v40, v48, v49\n"
    "v_cvt_pk_bf16_f32 v41, v50, v51\n"
    "v_cvt_pk_bf16_f32 v42, v52, v53\n"
    "v_cvt_pk_bf16_f32 v43, v54, v55\n"
    "v_cvt_pk_bf16_f32 v44, v56, v57\n"
    "v_cvt_pk_bf16_f32 v45, v58, v59\n"
    "v_cvt_pk_bf16_f32 v46, v60, v61\n"
    "v_cvt_pk_bf16_f32 v47, v62, v63\n"
    "v_mfma_f32_32x32x16_bf16 v[64:79], v[88:91], v[40:43], v[64:79]\n"
    "s_nop 0\n"
    "v_mfma_f32_32x32x16_bf16 v[64:79], v[92:95], v[44:47], v[64:79]\n"
    "s_nop 15\n"
    "s_cmp_lt_u32 s65, 2\n"
    "s_cbranch_scc1 .Lk_pe_16\n"
    "ds_read_b128 v[80:83], v19 offset:24576\n"
    "ds_read_b64_tr_b16 v[88:89], v162\n"
    "ds_read_b64_tr_b16 v[90:91], v162 offset:256\n"
    "ds_read_b64_tr_b16 v[92:93], v162 offset:512\n"
    "ds_read_b64_tr_b16 v[94:95], v162 offset:768\n"
    "s_waitcnt lgkmcnt(0)\n"
    "v_mfma_f32_32x32x16_bf16 v[48:63], v[80:83], v[28:31], 0\n"
    "s_nop 11\n"
    "v_sub_f32 v48, v48, v169\n"
    "v_sub_f32 v49, v49, v169\n"
    "v_sub_f32 v50, v50, v169\n"
    "v_sub_f32 v51, v51, v169\n"
    "v_sub_f32 v52, v52, v169\n"
    "v_sub_f32 v53, v53, v169\n"
    "v_sub_f32 v54, v54, v169\n"
    "v_sub_f32 v55, v55, v169\n"
    "v_sub_f32 v56, v56, v169\n"
    "v_sub_f32 v57, v57, v169\n"
    "v_sub_f32 v58, v58, v169\n"
    "v_sub_f32 v59, v59, v169\n"
    "v_sub_f32 v60, v60, v169\n"
    "v_sub_f32 v61, v61, v169\n"
    "v_sub_f32 v62, v62, v169\n"
    "v_sub_f32 v63, v63, v169\n"
    "v_exp_f32 v48, v48\n"
    "v_exp_f32 v49, v49\n"
    "v_exp_f32 v50, v50\n"
    "v_exp_f32 v51, v51\n"
    "v_exp_f32 v52, v52\n"
    "v_exp_f32 v53, v53\n"
    "v_exp_f32 v54, v54\n"
    "v_exp_f32 v55, v55\n"
    "v_exp_f32 v56, v56\n"
    "v_exp_f32 v57, v57\n"
    "v_exp_f32 v58, v58\n"
    "v_exp_f32 v59, v59\n"
    "v_exp_f32 v60, v60\n"
    "v_exp_f32 v61, v61\n"
    "v_exp_f32 v62, v62\n"
    "v_exp_f32 v63, v63\n"
    "v_cvt_pk_bf16_f32 v40, v48, v49\n"
    "v_cvt_pk_bf16_f32 v41, v50, v51\n"
    "v_cvt_pk_bf16_f32 v42, v52, v53\n"
    "v_cvt_pk_bf16_f32 v43, v54, v55\n"
    "v_cvt_pk_bf16_f32 v44, v56, v57\n"
    "v_cvt_pk_bf16_f32 v45, v58, v59\n"
    "v_cvt_pk_bf16_f32 v46, v60, v61\n"
    "v_cvt_pk_bf16_f32 v47, v62, v63\n"
    "v_mfma_f32_32x32x16_bf16 v[64:79], v[88:91], v[40:43], v[64:79]\n"
    "s_nop 0\n"
    "v_mfma_f32_32x32x16_bf16 v[64:79], v[92:95], v[44:47], v[64:79]\n"
    "s_nop 15\n"
    "s_cmp_lt_u32 s65, 3\n"
    "s_cbranch_scc1 .Lk_pe_16\n"
    "ds_read_b128 v[80:83], v19 offset:20480\n"
    "ds_read_b64_tr_b16 v[88:89], v163\n"
    "ds_read_b64_tr_b16 v[90:91], v163 offset:256\n"
    "ds_read_b64_tr_b16 v[92:93], v163 offset:512\n"
    "ds_read_b64_tr_b16 v[94:95], v163 offset:768\n"
    "s_waitcnt lgkmcnt(0)\n"
    "v_mfma_f32_32x32x16_bf16 v[48:63], v[80:83], v[28:31], 0\n"
    "s_nop 11\n"
    "v_sub_f32 v48, v48, v169\n"
    "v_sub_f32 v49, v49, v169\n"
    "v_sub_f32 v50, v50, v169\n"
    "v_sub_f32 v51, v51, v169\n"
    "v_sub_f32 v52, v52, v169\n"
    "v_sub_f32 v53, v53, v169\n"
    "v_sub_f32 v54, v54, v169\n"
    "v_sub_f32 v55, v55, v169\n"
    "v_sub_f32 v56, v56, v169\n"
    "v_sub_f32 v57, v57, v169\n"
    "v_sub_f32 v58, v58, v169\n"
    "v_sub_f32 v59, v59, v169\n"
    "v_sub_f32 v60, v60, v169\n"
    "v_sub_f32 v61, v61, v169\n"
    "v_sub_f32 v62, v62, v169\n"
    "v_sub_f32 v63, v63, v169\n"
    "v_exp_f32 v48, v48\n"
    "v_exp_f32 v49, v49\n"
    "v_exp_f32 v50, v50\n"
    "v_exp_f32 v51, v51\n"
    "v_exp_f32 v52, v52\n"
    "v_exp_f32 v53, v53\n"
    "v_exp_f32 v54, v54\n"
    "v_exp_f32 v55, v55\n"
    "v_exp_f32 v56, v56\n"
    "v_exp_f32 v57, v57\n"
    "v_exp_f32 v58, v58\n"
    "v_exp_f32 v59, v59\n"
    "v_exp_f32 v60, v60\n"
    "v_exp_f32 v61, v61\n"
    "v_exp_f32 v62, v62\n"
    "v_exp_f32 v63, v63\n"
    "v_cvt_pk_bf16_f32 v40, v48, v49\n"
    "v_cvt_pk_bf16_f32 v41, v50, v51\n"
    "v_cvt_pk_bf16_f32 v42, v52, v53\n"
    "v_cvt_pk_bf16_f32 v43, v54, v55\n"
    "v_cvt_pk_bf16_f32 v44, v56, v57\n"
    "v_cvt_pk_bf16_f32 v45, v58, v59\n"
    "v_cvt_pk_bf16_f32 v46, v60, v61\n"
    "v_cvt_pk_bf16_f32 v47, v62, v63\n"
    "v_mfma_f32_32x32x16_bf16 v[64:79], v[88:91], v[40:43], v[64:79]\n"
    "s_nop 0\n"
    "v_mfma_f32_32x32x16_bf16 v[64:79], v[92:95], v[44:47], v[64:79]\n"
    "s_nop 15\n"
    "s_cmp_lt_u32 s65, 4\n"
    "s_cbranch_scc1 .Lk_pe_16\n"
    "ds_read_b128 v[80:83], v19 offset:16384\n"
    "ds_read_b64_tr_b16 v[88:89], v164\n"
    "ds_read_b64_tr_b16 v[90:91], v164 offset:256\n"
    "ds_read_b64_tr_b16 v[92:93], v164 offset:512\n"
    "ds_read_b64_tr_b16 v[94:95], v164 offset:768\n"
    "s_waitcnt lgkmcnt(0)\n"
    "v_mfma_f32_32x32x16_bf16 v[48:63], v[80:83], v[28:31], 0\n"
    "s_nop 11\n"
    "v_sub_f32 v48, v48, v169\n"
    "v_sub_f32 v49, v49, v169\n"
    "v_sub_f32 v50, v50, v169\n"
    "v_sub_f32 v51, v51, v169\n"
    "v_sub_f32 v52, v52, v169\n"
    "v_sub_f32 v53, v53, v169\n"
    "v_sub_f32 v54, v54, v169\n"
    "v_sub_f32 v55, v55, v169\n"
    "v_sub_f32 v56, v56, v169\n"
    "v_sub_f32 v57, v57, v169\n"
    "v_sub_f32 v58, v58, v169\n"
    "v_sub_f32 v59, v59, v169\n"
    "v_sub_f32 v60, v60, v169\n"
    "v_sub_f32 v61, v61, v169\n"
    "v_sub_f32 v62, v62, v169\n"
    "v_sub_f32 v63, v63, v169\n"
    "v_exp_f32 v48, v48\n"
    "v_exp_f32 v49, v49\n"
    "v_exp_f32 v50, v50\n"
    "v_exp_f32 v51, v51\n"
    "v_exp_f32 v52, v52\n"
    "v_exp_f32 v53, v53\n"
    "v_exp_f32 v54, v54\n"
    "v_exp_f32 v55, v55\n"
    "v_exp_f32 v56, v56\n"
    "v_exp_f32 v57, v57\n"
    "v_exp_f32 v58, v58\n"
    "v_exp_f32 v59, v59\n"
    "v_exp_f32 v60, v60\n"
    "v_exp_f32 v61, v61\n"
    "v_exp_f32 v62, v62\n"
    "v_exp_f32 v63, v63\n"
    "v_cvt_pk_bf16_f32 v40, v48, v49\n"
    "v_cvt_pk_bf16_f32 v41, v50, v51\n"
    "v_cvt_pk_bf16_f32 v42, v52, v53\n"
    "v_cvt_pk_bf16_f32 v43, v54, v55\n"
    "v_cvt_pk_bf16_f32 v44, v56, v57\n"
    "v_cvt_pk_bf16_f32 v45, v58, v59\n"
    "v_cvt_pk_bf16_f32 v46, v60, v61\n"
    "v_cvt_pk_bf16_f32 v47, v62, v63\n"
    "v_mfma_f32_32x32x16_bf16 v[64:79], v[88:91], v[40:43], v[64:79]\n"
    "s_nop 0\n"
    "v_mfma_f32_32x32x16_bf16 v[64:79], v[92:95], v[44:47], v[64:79]\n"
    "s_nop 15\n"
    "s_cmp_lt_u32 s65, 5\n"
    "s_cbranch_scc1 .Lk_pe_16\n"
    "ds_read_b128 v[80:83], v19 offset:12288\n"
    "ds_read_b64_tr_b16 v[88:89], v165\n"
    "ds_read_b64_tr_b16 v[90:91], v165 offset:256\n"
    "ds_read_b64_tr_b16 v[92:93], v165 offset:512\n"
    "ds_read_b64_tr_b16 v[94:95], v165 offset:768\n"
    "s_waitcnt lgkmcnt(0)\n"
    "v_mfma_f32_32x32x16_bf16 v[48:63], v[80:83], v[28:31], 0\n"
    "s_nop 11\n"
    "v_sub_f32 v48, v48, v169\n"
    "v_sub_f32 v49, v49, v169\n"
    "v_sub_f32 v50, v50, v169\n"
    "v_sub_f32 v51, v51, v169\n"
    "v_sub_f32 v52, v52, v169\n"
    "v_sub_f32 v53, v53, v169\n"
    "v_sub_f32 v54, v54, v169\n"
    "v_sub_f32 v55, v55, v169\n"
    "v_sub_f32 v56, v56, v169\n"
    "v_sub_f32 v57, v57, v169\n"
    "v_sub_f32 v58, v58, v169\n"
    "v_sub_f32 v59, v59, v169\n"
    "v_sub_f32 v60, v60, v169\n"
    "v_sub_f32 v61, v61, v169\n"
    "v_sub_f32 v62, v62, v169\n"
    "v_sub_f32 v63, v63, v169\n"
    "v_exp_f32 v48, v48\n"
    "v_exp_f32 v49, v49\n"
    "v_exp_f32 v50, v50\n"
    "v_exp_f32 v51, v51\n"
    "v_exp_f32 v52, v52\n"
    "v_exp_f32 v53, v53\n"
    "v_exp_f32 v54, v54\n"
    "v_exp_f32 v55, v55\n"
    "v_exp_f32 v56, v56\n"
    "v_exp_f32 v57, v57\n"
    "v_exp_f32 v58, v58\n"
    "v_exp_f32 v59, v59\n"
    "v_exp_f32 v60, v60\n"
    "v_exp_f32 v61, v61\n"
    "v_exp_f32 v62, v62\n"
    "v_exp_f32 v63, v63\n"
    "v_cvt_pk_bf16_f32 v40, v48, v49\n"
    "v_cvt_pk_bf16_f32 v41, v50, v51\n"
    "v_cvt_pk_bf16_f32 v42, v52, v53\n"
    "v_cvt_pk_bf16_f32 v43, v54, v55\n"
    "v_cvt_pk_bf16_f32 v44, v56, v57\n"
    "v_cvt_pk_bf16_f32 v45, v58, v59\n"
    "v_cvt_pk_bf16_f32 v46, v60, v61\n"
    "v_cvt_pk_bf16_f32 v47, v62, v63\n"
    "v_mfma_f32_32x32x16_bf16 v[64:79], v[88:91], v[40:43], v[64:79]\n"
    "s_nop 0\n"
    "v_mfma_f32_32x32x16_bf16 v[64:79], v[92:95], v[44:47], v[64:79]\n"
    "s_nop 15\n"
    "s_cmp_lt_u32 s65, 6\n"
    "s_cbranch_scc1 .Lk_pe_16\n"
    "ds_read_b128 v[80:83], v19 offset:8192\n"
    "ds_read_b64_tr_b16 v[88:89], v166\n"
    "ds_read_b64_tr_b16 v[90:91], v166 offset:256\n"
    "ds_read_b64_tr_b16 v[92:93], v166 offset:512\n"
    "ds_read_b64_tr_b16 v[94:95], v166 offset:768\n"
    "s_waitcnt lgkmcnt(0)\n"
    "v_mfma_f32_32x32x16_bf16 v[48:63], v[80:83], v[28:31], 0\n"
    "s_nop 11\n"
    "v_sub_f32 v48, v48, v169\n"
    "v_sub_f32 v49, v49, v169\n"
    "v_sub_f32 v50, v50, v169\n"
    "v_sub_f32 v51, v51, v169\n"
    "v_sub_f32 v52, v52, v169\n"
    "v_sub_f32 v53, v53, v169\n"
    "v_sub_f32 v54, v54, v169\n"
    "v_sub_f32 v55, v55, v169\n"
    "v_sub_f32 v56, v56, v169\n"
    "v_sub_f32 v57, v57, v169\n"
    "v_sub_f32 v58, v58, v169\n"
    "v_sub_f32 v59, v59, v169\n"
    "v_sub_f32 v60, v60, v169\n"
    "v_sub_f32 v61, v61, v169\n"
    "v_sub_f32 v62, v62, v169\n"
    "v_sub_f32 v63, v63, v169\n"
    "v_exp_f32 v48, v48\n"
    "v_exp_f32 v49, v49\n"
    "v_exp_f32 v50, v50\n"
    "v_exp_f32 v51, v51\n"
    "v_exp_f32 v52, v52\n"
    "v_exp_f32 v53, v53\n"
    "v_exp_f32 v54, v54\n"
    "v_exp_f32 v55, v55\n"
    "v_exp_f32 v56, v56\n"
    "v_exp_f32 v57, v57\n"
    "v_exp_f32 v58, v58\n"
    "v_exp_f32 v59, v59\n"
    "v_exp_f32 v60, v60\n"
    "v_exp_f32 v61, v61\n"
    "v_exp_f32 v62, v62\n"
    "v_exp_f32 v63, v63\n"
    "v_cvt_pk_bf16_f32 v40, v48, v49\n"
    "v_cvt_pk_bf16_f32 v41, v50, v51\n"
    "v_cvt_pk_bf16_f32 v42, v52, v53\n"
    "v_cvt_pk_bf16_f32 v43, v54, v55\n"
    "v_cvt_pk_bf16_f32 v44, v56, v57\n"
    "v_cvt_pk_bf16_f32 v45, v58, v59\n"
    "v_cvt_pk_bf16_f32 v46, v60, v61\n"
    "v_cvt_pk_bf16_f32 v47, v62, v63\n"
    "v_mfma_f32_32x32x16_bf16 v[64:79], v[88:91], v[40:43], v[64:79]\n"
    "s_nop 0\n"
    "v_mfma_f32_32x32x16_bf16 v[64:79], v[92:95], v[44:47], v[64:79]\n"
    "s_nop 15\n"
    "s_cmp_lt_u32 s65, 7\n"
    "s_cbranch_scc1 .Lk_pe_16\n"
    "ds_read_b128 v[80:83], v19 offset:4096\n"
    "ds_read_b64_tr_b16 v[88:89], v167\n"
    "ds_read_b64_tr_b16 v[90:91], v167 offset:256\n"
    "ds_read_b64_tr_b16 v[92:93], v167 offset:512\n"
    "ds_read_b64_tr_b16 v[94:95], v167 offset:768\n"
    "s_waitcnt lgkmcnt(0)\n"
    "v_mfma_f32_32x32x16_bf16 v[48:63], v[80:83], v[28:31], 0\n"
    "s_nop 11\n"
    "v_sub_f32 v48, v48, v169\n"
    "v_sub_f32 v49, v49, v169\n"
    "v_sub_f32 v50, v50, v169\n"
    "v_sub_f32 v51, v51, v169\n"
    "v_sub_f32 v52, v52, v169\n"
    "v_sub_f32 v53, v53, v169\n"
    "v_sub_f32 v54, v54, v169\n"
    "v_sub_f32 v55, v55, v169\n"
    "v_sub_f32 v56, v56, v169\n"
    "v_sub_f32 v57, v57, v169\n"
    "v_sub_f32 v58, v58, v169\n"
    "v_sub_f32 v59, v59, v169\n"
    "v_sub_f32 v60, v60, v169\n"
    "v_sub_f32 v61, v61, v169\n"
    "v_sub_f32 v62, v62, v169\n"
    "v_sub_f32 v63, v63, v169\n"
    "v_exp_f32 v48, v48\n"
    "v_exp_f32 v49, v49\n"
    "v_exp_f32 v50, v50\n"
    "v_exp_f32 v51, v51\n"
    "v_exp_f32 v52, v52\n"
    "v_exp_f32 v53, v53\n"
    "v_exp_f32 v54, v54\n"
    "v_exp_f32 v55, v55\n"
    "v_exp_f32 v56, v56\n"
    "v_exp_f32 v57, v57\n"
    "v_exp_f32 v58, v58\n"
    "v_exp_f32 v59, v59\n"
    "v_exp_f32 v60, v60\n"
    "v_exp_f32 v61, v61\n"
    "v_exp_f32 v62, v62\n"
    "v_exp_f32 v63, v63\n"
    "v_cvt_pk_bf16_f32 v40, v48, v49\n"
    "v_cvt_pk_bf16_f32 v41, v50, v51\n"
    "v_cvt_pk_bf16_f32 v42, v52, v53\n"
    "v_cvt_pk_bf16_f32 v43, v54, v55\n"
    "v_cvt_pk_bf16_f32 v44, v56, v57\n"
    "v_cvt_pk_bf16_f32 v45, v58, v59\n"
    "v_cvt_pk_bf16_f32 v46, v60, v61\n"
    "v_cvt_pk_bf16_f32 v47, v62, v63\n"
    "v_mfma_f32_32x32x16_bf16 v[64:79], v[88:91], v[40:43], v[64:79]\n"
    "s_nop 0\n"
    "v_mfma_f32_32x32x16_bf16 v[64:79], v[92:95], v[44:47], v[64:79]\n"
    "s_nop 15\n"
    "s_cmp_lt_u32 s65, 8\n"
    "s_cbranch_scc1 .Lk_pe_16\n"
    "ds_read_b128 v[80:83], v19\n"
    "ds_read_b64_tr_b16 v[88:89], v168\n"
    "ds_read_b64_tr_b16 v[90:91], v168 offset:256\n"
    "ds_read_b64_tr_b16 v[92:93], v168 offset:512\n"
    "ds_read_b64_tr_b16 v[94:95], v168 offset:768\n"
    "s_waitcnt lgkmcnt(0)\n"
    "v_mfma_f32_32x32x16_bf16 v[48:63], v[80:83], v[28:31], v[144:159]\n"
    "s_nop 11\n"
    "v_sub_f32 v48, v48, v169\n"
    "v_sub_f32 v49, v49, v169\n"
    "v_sub_f32 v50, v50, v169\n"
    "v_sub_f32 v51, v51, v169\n"
    "v_sub_f32 v52, v52, v169\n"
    "v_sub_f32 v53, v53, v169\n"
    "v_sub_f32 v54, v54, v169\n"
    "v_sub_f32 v55, v55, v169\n"
    "v_sub_f32 v56, v56, v169\n"
    "v_sub_f32 v57, v57, v169\n"
    "v_sub_f32 v58, v58, v169\n"
    "v_sub_f32 v59, v59, v169\n"
    "v_sub_f32 v60, v60, v169\n"
    "v_sub_f32 v61, v61, v169\n"
    "v_sub_f32 v62, v62, v169\n"
    "v_sub_f32 v63, v63, v169\n"
    "v_exp_f32 v48, v48\n"
    "v_exp_f32 v49, v49\n"
    "v_exp_f32 v50, v50\n"
    "v_exp_f32 v51, v51\n"
    "v_exp_f32 v52, v52\n"
    "v_exp_f32 v53, v53\n"
    "v_exp_f32 v54, v54\n"
    "v_exp_f32 v55, v55\n"
    "v_exp_f32 v56, v56\n"
    "v_exp_f32 v57, v57\n"
    "v_exp_f32 v58, v58\n"
    "v_exp_f32 v59, v59\n"
    "v_exp_f32 v60, v60\n"
    "v_exp_f32 v61, v61\n"
    "v_exp_f32 v62, v62\n"
    "v_exp_f32 v63, v63\n"
    "v_cvt_pk_bf16_f32 v40, v48, v49\n"
    "v_cvt_pk_bf16_f32 v41, v50, v51\n"
    "v_cvt_pk_bf16_f32 v42, v52, v53\n"
    "v_cvt_pk_bf16_f32 v43, v54, v55\n"
    "v_cvt_pk_bf16_f32 v44, v56, v57\n"
    "v_cvt_pk_bf16_f32 v45, v58, v59\n"
    "v_cvt_pk_bf16_f32 v46, v60, v61\n"
    "v_cvt_pk_bf16_f32 v47, v62, v63\n"
    "v_mfma_f32_32x32x16_bf16 v[64:79], v[88:91], v[40:43], v[64:79]\n"
    "s_nop 0\n"
    "v_mfma_f32_32x32x16_bf16 v[64:79], v[92:95], v[44:47], v[64:79]\n"
    "s_nop 15\n"
    ".Lk_pe_16:\n"
    "s_nop 15\n"
    "s_branch .Lk_norm\n"
    "s_nop 7\n"
    ".Lk_spin_9:\n"
    "s_sleep 1\n"
    "s_add_u32 s71, s62, 0x18000\n"
    "buffer_load_dword v124, v2, s[28:31], s71 offen sc1\n"
    "s_waitcnt vmcnt(0)\n"
    "v_cmp_ne_u32 vcc, v22, v124\n"
    "s_add_u32 s74, s74, 1\n"
    "s_cbranch_vccz .Lk_ok_10\n"
    "s_cmp_lt_u32 s74, 0x20000\n"
    "s_cbranch_scc1 .Lk_spin_9\n"
    "s_branch .Lk_ok_10\n"
    "s_nop 7\n"
    ".Lk_spin_11:\n"
    "s_sleep 1\n"
    "s_add_u32 s71, s62, 0x20000\n"
    "buffer_load_dword v124, v2, s[28:31], s71 offen sc1\n"
    "s_waitcnt vmcnt(0)\n"
    "v_cmp_ne_u32 vcc, v22, v124\n"
    "s_add_u32 s74, s74, 1\n"
    "s_cbranch_vccz .Lk_ok_12\n"
    "s_cmp_lt_u32 s74, 0x20000\n"
    "s_cbranch_scc1 .Lk_spin_11\n"
    "s_branch .Lk_ok_12\n"
    "s_nop 7\n"
    ".Lk_eug_14:\n"
    "v_lshrrev_b32 v152, 2, v0\n"
    "v_lshlrev_b32 v152, 4, v152\n"
    "v_and_b32 v144, 3, v0\n"
    "v_add_u32 v152, v152, v144\n"
    "v_lshlrev_b32 v152, 7, v152\n"
    ".Lk_eur_17:\n"
    "s_mov_b32 s70, 0x8000\n"
    "buffer_load_dwordx4 v[144:147], v152, s[28:31], s70 offen sc1\n"
    "buffer_load_dwordx4 v[148:151], v152, s[28:31], s70 offen offset:16 sc1\n"
    "s_waitcnt vmcnt(0)\n"
    "v_xor_b32 v144, v22, v144\n"
    "v_xor_b32 v145, v22, v145\n"
    "v_xor_b32 v146, v22, v146\n"
    "v_xor_b32 v147, v22, v147\n"
    "v_xor_b32 v148, v22, v148\n"
    "v_xor_b32 v149, v22, v149\n"
    "v_xor_b32 v150, v22, v150\n"
    "v_xor_b32 v151, v22, v151\n"
    "v_or_b32 v144, v144, v145\n"
    "v_or_b32 v144, v144, v146\n"
    "v_or_b32 v144, v144, v147\n"
    "v_or_b32 v144, v144, v148\n"
    "v_or_b32 v144, v144, v149\n"
    "v_or_b32 v144, v144, v150\n"
    "v_or_b32 v144, v144, v151\n"
    "v_cmp_ne_u32 vcc, 0, v144\n"
    "s_cbranch_vccz .Lk_eud_18\n"
    "s_add_u32 s74, s74, 1\n"
    "s_cmp_lt_u32 s74, 0x20000\n"
    "s_cbranch_scc1 .Lk_eur_17\n"
    ".Lk_eud_18:\n"
    "s_mov_b64 exec, 1\n"
    "buffer_store_dword v22, off, s[28:31], 0 sc1\n"
    "s_mov_b64 exec, -1\n"
    "s_branch .Lk_eu_13\n"
    ".Lk_end:\n"
    : "+v"(tid)
    : "s"(__builtin_amdgcn_kernarg_segment_ptr()), "s"(blockIdx.x), "s"((unsigned)(size_t)lds)
    : "v1", "v2", "v3", "v4", "v5", "v6", "v7", "v8", "v9", "v10", "v11", "v12", "v13", "v14", "v15", "v16", "v17", "v18", "v19", "v20", "v21", "v22", "v23", "v24", "v25", "v26", "v27", "v28", "v29", "v30", "v31", "v32", "v33", "v34", "v35", "v36", "v37", "v38", "v39", "v40", "v41", "v42", "v43", "v44", "v45", "v46", "v47", "v48", "v49", "v50", "v51", "v52", "v53", "v54", "v55", "v56", "v57", "v58", "v59", "v60", "v61", "v62", "v63", "v64", "v65", "v66", "v67", "v68", "v69", "v70", "v71", "v72", "v73", "v74", "v75", "v76", "v77", "v78", "v79", "v80", "v81", "v82", "v83", "v84", "v85", "v86", "v87", "v88", "v89", "v90", "v91", "v92", "v93", "v94", "v95", "v96", "v97", "v98", "v99", "v100", "v101", "v102", "v103", "v104", "v105", "v106", "v107", "v108", "v109", "v110", "v111", "v112", "v113", "v114", "v115", "v116", "v117", "v118", "v119", "v120", "v121", "v122", "v123", "v124", "v125", "v126", "v127", "v128", "v129", "v130", "v131", "v132", "v133", "v134", "v135", "v136", "v137", "v138", "v139", "v140", "v141", "v142", "v143", "v144", "v145", "v146", "v147", "v148", "v149", "v150", "v151", "v152", "v153", "v154", "v155", "v156", "v157", "v158", "v159", "v160", "v161", "v162", "v163", "v164", "v165", "v166", "v167", "v168", "v169", "v170", "v171", "v172", "v173", "v174", "v175", "v176", "v177", "v178", "v179", "v180", "v181", "v182", "v183", "v184", "v185", "v186", "v187", "v188", "v189", "v190", "v191", "v192", "v193", "v194", "v195", "v196", "v197", "v198", "v199", "v200", "v201", "v202", "v203", "v204", "v205", "v206", "v207", "v208", "v209", "v210", "v211", "v212", "v213", "v214", "v215", "v216", "v217", "v218", "v219", "v220", "v221", "v222", "v223", "v224", "v225", "v226", "v227", "v228", "v229", "v230", "v231", "v232", "v233", "v234", "v235", "v236", "v237", "v238", "v239", "v240", "v241", "v242", "v243", "v244", "v245", "v246", "v247", "v248", "v249", "v250", "v251", "v252", "v253", "v254", "v255", "s4", "s5", "s6", "s7", "s8", "s9", "s10", "s11", "s12", "s13", "s14", "s15", "s16", "s17", "s18", "s19", "s20", "s21", "s22", "s23", "s24", "s25", "s26", "s27", "s28", "s29", "s30", "s31", "s33", "s34", "s35", "s36", "s37", "s38", "s39", "s40", "s41", "s42", "s43", "s44", "s45", "s46", "s47", "s48", "s49", "s50", "s51", "s52", "s53", "s54", "s55", "s56", "s57", "s58", "s59", "s60", "s61", "s62", "s63", "s64", "s65", "s66", "s67", "s68", "s69", "s70", "s71", "s72", "s73", "s74", "s75", "s76", "s77", "s78", "s79", "s80", "s81", "s82", "s83", "s84", "s85", "s86", "s87", "s88", "s89", "s90", "s91", "s92", "s93", "s94", "s95", "s96", "s97", "s98", "s99", "s100", "s101", "vcc", "memory");
}

extern "C" void kernel_launch(void* const* d_in, const int* in_sizes, int n_in, void* d_out, int out_size, void* d_ws, size_t ws_size,
                              hipStream_t stream) {
  fused_block_kernel<<<256, 512, 0, stream>>>(d_in[0], d_in[1], d_in[2], d_in[4], d_in[5], d_out, d_ws);
}
```
